```python
import torch

def _softmax(x, axis=-1):
    return torch.softmax(x, dim=axis)

def _gelu(x):
    return torch.nn.functional.gelu(x, approximate='tanh')

def _silu(x):
    return torch.nn.functional.silu(x)

def _sigmoid(x):
    return torch.sigmoid(x)

def _layernorm(x, axis=-1, eps=1e-05):
    m = x.mean(dim=axis, keepdim=True)
    v = x.var(dim=axis, keepdim=True, unbiased=False)
    return (x - m) / torch.sqrt(v + eps)

def _scatter_add(tgt, idx, src, axis=0):
    return tgt.clone().index_add_(axis, idx, src)

def _segment_sum(src, seg, n):
    out = torch.zeros((n,) + src.shape[1:], dtype=src.dtype)
    return out.index_add_(0, seg, src)

def _pad(x, pw):
    flat = tuple((v for pair in reversed(pw) for v in pair))
    return torch.nn.functional.pad(x, flat)
kernel_name = 'adaLN_cond__compressed_kv_cla__glu_gated_geglu__decode'

def setup_inputs():

    _w = lambda a: (lambda t: t.to(torch.bfloat16) if t.is_floating_point() else t)(torch.as_tensor(a))

    def _A_setup():
        B, H, Sq, Sk, D, M = (2, 32, 1, 8192, 128, 512)
        Dc = 4 * D
        return {'x': _w((lambda *s: torch.randn(*s, dtype=torch.bfloat16) * 0.05)(B, Sq, M)), 'c_kv': _w((lambda *s: torch.randn(*s, dtype=torch.bfloat16) * 3.2)(B, Sk, Dc)), 'w_q': _w((lambda *s: torch.randn(*s, dtype=torch.bfloat16) * 0.8)(M, H, D) * M ** (-0.5)), 'w_uk': _w((lambda *s: torch.randn(*s, dtype=torch.bfloat16) * 0.8)(Dc, H, D) * Dc ** (-0.5)), 'w_uv': _w((lambda *s: torch.randn(*s, dtype=torch.bfloat16) * 3.2)(Dc, H, D) * Dc ** (-0.5)), 'w_o': _w((lambda *s: torch.randn(*s, dtype=torch.bfloat16) * 3.2)(H, D, M) * (H * D) ** (-0.5))}
    _A_in = _A_setup()

    def _B_setup():
        B, S, Dm, Df = (2, 1, 512, 1792)
        return {'x': _w((lambda *s: torch.randn(*s, dtype=torch.bfloat16) * 0.05)(B, S, Dm)), 'w_gate': _w((lambda *s: torch.randn(*s, dtype=torch.bfloat16) * 3.2)(Dm, Df) * Dm ** (-0.5)), 'w_up': _w((lambda *s: torch.randn(*s, dtype=torch.bfloat16) * 3.2)(Dm, Df) * Dm ** (-0.5)), 'w_down': _w((lambda *s: torch.randn(*s, dtype=torch.bfloat16) * 3.2)(Df, Dm) * Df ** (-0.5))}
    _B_in = _B_setup()
    return {'A_x': _A_in['x'], 'A_c_kv': _A_in['c_kv'], 'A_w_q': _A_in['w_q'], 'A_w_uk': _A_in['w_uk'], 'A_w_uv': _A_in['w_uv'], 'A_w_o': _A_in['w_o'], 'B_x': _B_in['x'], 'B_w_gate': _B_in['w_gate'], 'B_w_up': _B_in['w_up'], 'B_w_down': _B_in['w_down'], 'c': _w((lambda *s: torch.randn(*s, dtype=torch.bfloat16) * 0.1)(2, 512) * 8.0), 'w_mod': _w((lambda *s: torch.randn(*s, dtype=torch.bfloat16) * 0.1)(512, 6 * 512) * 512 ** (-0.5) * 8.0)}

def reference(A_x, A_c_kv, A_w_q, A_w_uk, A_w_uv, A_w_o, B_x, B_w_gate, B_w_up, B_w_down, c, w_mod):

    def _A_ref(x, c_kv, w_q, w_uk, w_uv, w_o):
        B, Sq, M = (x.shape[0], x.shape[1], x.shape[2])
        Sk = c_kv.shape[1]
        H, D = (w_q.shape[1], w_q.shape[2])
        q = torch.einsum('bqm,mhd->bhqd', x, w_q)
        k = torch.einsum('bkc,chd->bhkd', c_kv, w_uk)
        v = torch.einsum('bkc,chd->bhkd', c_kv, w_uv)
        scores = torch.einsum('bhqd,bhkd->bhqk', q, k) * D ** (-0.5)
        off = Sk - Sq
        qi = torch.arange(Sq)[:, None]
        kj = torch.arange(Sk)[None, :]
        cmask = kj <= qi + off
        scores = torch.where(cmask[None, None], scores, -1000000000.0)
        p = _softmax(scores, axis=-1)
        ctx = torch.einsum('bhqk,bhkd->bhqd', p, v)
        return torch.einsum('bhqd,hdm->bqm', ctx, w_o)

    def _B_ref(x, w_gate, w_up, w_down):
        g = torch.einsum('bsd,df->bsf', x, w_gate)
        u = torch.einsum('bsd,df->bsf', x, w_up)
        h = _gelu(g) * u
        return torch.einsum('bsf,fd->bsd', h, w_down)
    x = A_x
    mod = (_silu(c) @ w_mod).reshape(c.shape[0], 6, 1, -1)
    sa, ca, ga, sb, cb, gb = (mod[:, 0].clone(), mod[:, 1].clone(), mod[:, 2].clone(), mod[:, 3].clone(), mod[:, 4].clone(), mod[:, 5].clone())
    h = _layernorm(x, axis=-1) * (1 + ca) + sa
    x = x + ga * _A_ref(h, A_c_kv, A_w_q, A_w_uk, A_w_uv, A_w_o)
    h = _layernorm(x, axis=-1) * (1 + cb) + sb
    x = x + gb * _B_ref(h, B_w_gate, B_w_up, B_w_down)
    return x
```

```cpp
#include <hip/hip_runtime.h>
#include <hip/hip_bf16.h>
typedef unsigned short u16;
__device__ __forceinline__ float bf2f(u16 v) { return __uint_as_float(((unsigned)v) << 16); }
__device__ __forceinline__ u16 f2bf(float f) { __hip_bfloat16 h = __float2bfloat16(f); return *reinterpret_cast<u16*>(&h); }
__device__ __forceinline__ float rbf(float f) { return bf2f(f2bf(f)); }

__global__ void k_mod(const u16* c, const u16* wmod, float* mod) {
  int n = blockIdx.x * 64 + threadIdx.x; int b = blockIdx.y;
  float acc = 0;
  for (int k = 0; k < 512; ++k) { float cv = bf2f(c[b * 512 + k]); float s = rbf(cv / (1.f + __expf(-cv))); acc += s * bf2f(wmod[(size_t)k * 3072 + n]); }
  mod[b * 3072 + n] = rbf(acc);
}
__device__ void ln_mod(const float* x, const float* shift, const float* scale, float* h, float* red) {
  int t = threadIdx.x;
  float v = x[t];
  red[t] = v; __syncthreads();
  for (int s = 256; s > 0; s >>= 1) { if (t < s) red[t] += red[t + s]; __syncthreads(); }
  float mean = red[0] / 512.f; __syncthreads();
  float d = v - mean; red[t] = d * d; __syncthreads();
  for (int s = 256; s > 0; s >>= 1) { if (t < s) red[t] += red[t + s]; __syncthreads(); }
  float var = red[0] / 512.f; __syncthreads();
  float ln = rbf(rbf(v - rbf(mean)) / rbf(sqrtf(rbf(rbf(var) + 1e-5f))));
  h[t] = rbf(rbf(ln * rbf(1.f + scale[t])) + shift[t]);
}
__global__ void k_q(const u16* x, const float* mod, const u16* wq, const u16* wuk, float* qabs) {
  __shared__ float xs[512], hs[512], red[512], qs[128];
  int h = blockIdx.x, b = blockIdx.y, t = threadIdx.x;
  xs[t] = bf2f(x[b * 512 + t]); __syncthreads();
  ln_mod(xs, mod + b * 3072, mod + b * 3072 + 512, hs, red); __syncthreads();
  if (t < 128) { float a = 0; for (int m = 0; m < 512; ++m) a += hs[m] * bf2f(wq[(size_t)m * 4096 + h * 128 + t]); qs[t] = rbf(a); }
  __syncthreads();
  float a = 0; for (int d = 0; d < 128; ++d) a += qs[d] * bf2f(wuk[(size_t)t * 4096 + h * 128 + d]);
  qabs[(b * 32 + h) * 512 + t] = rbf(a * 0.08838834764f);
}
__global__ void k_scores(const float* qabs, const u16* ckv, float* sc) {
  int b = blockIdx.y; int key = blockIdx.x * 8 + threadIdx.x / 32; int h = threadIdx.x % 32;
  float a = 0; const u16* kr = ckv + ((size_t)b * 8192 + key) * 512; const float* q = qabs + (b * 32 + h) * 512;
  for (int c = 0; c < 512; ++c) a += q[c] * bf2f(kr[c]);
  sc[((size_t)b * 32 + h) * 8192 + key] = a;
}
__global__ void k_soft(float* sc) {
  __shared__ float red[256];
  float* s = sc + (size_t)blockIdx.x * 8192; int t = threadIdx.x;
  float m = -1e30f; for (int k = t; k < 8192; k += 256) m = fmaxf(m, s[k]);
  red[t] = m; __syncthreads(); for (int o = 128; o > 0; o >>= 1) { if (t < o) red[t] = fmaxf(red[t], red[t + o]); __syncthreads(); }
  m = red[0]; __syncthreads();
  float l = 0; for (int k = t; k < 8192; k += 256) { float e = __expf(s[k] - m); s[k] = e; l += e; }
  red[t] = l; __syncthreads(); for (int o = 128; o > 0; o >>= 1) { if (t < o) red[t] += red[t + o]; __syncthreads(); }
  l = red[0];
  for (int k = t; k < 8192; k += 256) s[k] = s[k] / l;
}
__global__ void k_pv(const float* sc, const u16* ckv, const u16* wuv, float* ctx) {
  __shared__ float cc[512];
  int h = blockIdx.x, b = blockIdx.y, t = threadIdx.x;
  const float* p = sc + ((size_t)b * 32 + h) * 8192;
  float a = 0; for (int k = 0; k < 8192; ++k) a += p[k] * bf2f(ckv[((size_t)b * 8192 + k) * 512 + t]);
  cc[t] = a; __syncthreads();
  if (t < 128) { float s = 0; for (int c = 0; c < 512; ++c) s += cc[c] * bf2f(wuv[(size_t)c * 4096 + h * 128 + t]); ctx[(b * 32 + h) * 128 + t] = rbf(s); }
}
__global__ void k_o(const float* ctx, const u16* wo, const u16* x, const float* mod, float* x1) {
  int b = blockIdx.y; int m = blockIdx.x * 64 + threadIdx.x;
  float a = 0; for (int k = 0; k < 4096; ++k) a += ctx[b * 4096 + k] * bf2f(wo[(size_t)k * 512 + m]);
  x1[b * 512 + m] = rbf(bf2f(x[b * 512 + m]) + rbf(mod[b * 3072 + 1024 + m] * rbf(a)));
}
__global__ void k_ffn1(const float* x1, const float* mod, const u16* wg, const u16* wu, float* act) {
  __shared__ float hs[512], red[512];
  int b = blockIdx.y, t = threadIdx.x;
  ln_mod(x1 + b * 512, mod + b * 3072 + 1536, mod + b * 3072 + 2048, hs, red); __syncthreads();
  int f = blockIdx.x * 512 + t; if (f >= 1792) return;
  float g = 0, u = 0; for (int m = 0; m < 512; ++m) { g += hs[m] * bf2f(wg[(size_t)m * 1792 + f]); u += hs[m] * bf2f(wu[(size_t)m * 1792 + f]); }
  g = rbf(g); u = rbf(u);
  float ge = rbf(0.5f * g * (1.f + tanhf(0.7978845608f * (g + 0.044715f * g * g * g))));
  act[b * 1792 + f] = rbf(ge * u);
}
__global__ void k_ffn2(const float* act, const u16* wd, const float* x1, const float* mod, u16* out) {
  int b = blockIdx.y; int m = blockIdx.x * 64 + threadIdx.x;
  float a = 0; for (int f = 0; f < 1792; ++f) a += act[b * 1792 + f] * bf2f(wd[(size_t)f * 512 + m]);
  out[b * 512 + m] = f2bf(x1[b * 512 + m] + rbf(mod[b * 3072 + 2560 + m] * rbf(a)));
}
extern "C" void kernel_launch(void* const* d_in, const int* in_sizes, int n_in, void* d_out, int out_size, void* d_ws, size_t ws_size, hipStream_t stream) {
  const u16 *x = (const u16*)d_in[0], *ckv = (const u16*)d_in[1], *wq = (const u16*)d_in[2], *wuk = (const u16*)d_in[3], *wuv = (const u16*)d_in[4], *wo = (const u16*)d_in[5];
  const u16 *wg = (const u16*)d_in[7], *wu = (const u16*)d_in[8], *wd = (const u16*)d_in[9], *c = (const u16*)d_in[10], *wmod = (const u16*)d_in[11];
  float* ws = (float*)d_ws;
  float *mod = ws, *qabs = ws + 8192, *sc = qabs + 32768, *ctx = sc + 2 * 32 * 8192, *x1 = ctx + 8192, *act = x1 + 1024;
  k_mod<<<dim3(48, 2), 64, 0, stream>>>(c, wmod, mod);
  k_q<<<dim3(32, 2), 512, 0, stream>>>(x, mod, wq, wuk, qabs);
  k_scores<<<dim3(1024, 2), 256, 0, stream>>>(qabs, ckv, sc);
  k_soft<<<64, 256, 0, stream>>>(sc);
  k_pv<<<dim3(32, 2), 512, 0, stream>>>(sc, ckv, wuv, ctx);
  k_o<<<dim3(8, 2), 64, 0, stream>>>(ctx, wo, x, mod, x1);
  k_ffn1<<<dim3(4, 2), 512, 0, stream>>>(x1, mod, wg, wu, act);
  k_ffn2<<<dim3(8, 2), 64, 0, stream>>>(act, wd, x1, mod, (u16*)d_out);
}
```

```cpp
#include <hip/hip_runtime.h>
typedef unsigned short u16;
typedef unsigned u32;
typedef __attribute__((ext_vector_type(4))) unsigned u32x4;
typedef __attribute__((ext_vector_type(2))) unsigned u32x2;
typedef __attribute__((ext_vector_type(4))) float f32x4;
typedef __attribute__((ext_vector_type(8))) __bf16 bf16x8;
typedef __attribute__((ext_vector_type(2))) __bf16 bf16x2;
typedef __attribute__((ext_vector_type(2))) float f32x2;
#define DEV __device__ __forceinline__

#define W_EPOCH 0
#define W_HG 1024
#define W_MG 8192
#define W_QB 32768
#define W_LSE 98304
#define W_XE 114688
#define W_DP 131072
#define W_FP 655360
#define W_CI 1048576
#define W_TS (1048576 + 4194304)
#define W_BYTES (W_TS + 256 * 32 * 8)
#define KVS 1056
#define L_QS 135168
#define L_HS 151808
#define L_RED 153856
#define L_MX 162048
#define L_LS 162304
#define L_QP 162560
#define L_TOTAL 163584
#define L_CX L_QS
#define L_EW (L_QS + 4608)
#define L_ML (L_QS + 5632)
#define SPIN_MAX 400000

#define TS(i)

DEV float bf2f(u32 v) { return __uint_as_float(v << 16); }
DEV float bflo(u32 v) { return __uint_as_float(v << 16); }
DEV float bfhi(u32 v) { return __uint_as_float(v & 0xffff0000u); }
DEV u32 pk2(float a, float b) { f32x2 v = {a, b}; bf16x2 r = __builtin_convertvector(v, bf16x2); return __builtin_bit_cast(u32, r); }
DEV float rbf(float a) { return bflo(pk2(a, 0.f)); }
DEV u32 canon(u32 v) { return v < 0xFFFFFFFEu ? v : 0xFFFFFFFEu; }
DEV f32x4 mfma(u32x4 a, u32x4 b, f32x4 c) { return __builtin_amdgcn_mfma_f32_16x16x32_bf16(__builtin_bit_cast(bf16x8, a), __builtin_bit_cast(bf16x8, b), c, 0, 0, 0); }
DEV void fence() { asm volatile("" ::: "memory"); }
DEV void bar() { asm volatile("s_waitcnt lgkmcnt(0)\n\ts_barrier" ::: "memory"); }
DEV void waitvm0() { asm volatile("s_waitcnt vmcnt(0)" ::: "memory"); }
DEV void waitlgkm0() { asm volatile("s_waitcnt lgkmcnt(0)" ::: "memory"); }
typedef __amdgpu_buffer_rsrc_t rsrc_t;
DEV rsrc_t mk(const void* p, u32 bytes) { return __builtin_amdgcn_make_buffer_rsrc((void*)p, 0, bytes, 0x00020000); }
DEV u32x4 ld128(rsrc_t r, u32 off) { return __builtin_amdgcn_raw_buffer_load_b128(r, off, 0, 0); }
DEV u32x2 ld64(rsrc_t r, u32 off) { return __builtin_amdgcn_raw_buffer_load_b64(r, off, 0, 0); }
DEV u32 ld32(rsrc_t r, u32 off) { return __builtin_amdgcn_raw_buffer_load_b32(r, off, 0, 0); }
DEV u32x4 cld128(rsrc_t r, u32 off) { return __builtin_amdgcn_raw_buffer_load_b128(r, off, 0, 16); }
DEV u32x2 cld64(rsrc_t r, u32 off) { return __builtin_amdgcn_raw_buffer_load_b64(r, off, 0, 16); }
DEV u32 cld32(rsrc_t r, u32 off) { return __builtin_amdgcn_raw_buffer_load_b32(r, off, 0, 16); }
DEV void cst128(rsrc_t r, u32 off, u32x4 v) { __builtin_amdgcn_raw_buffer_store_b128(v, r, off, 0, 16); }
DEV void cst64(rsrc_t r, u32 off, u32x2 v) { __builtin_amdgcn_raw_buffer_store_b64(v, r, off, 0, 16); }
DEV void cst32(rsrc_t r, u32 off, u32 v) { __builtin_amdgcn_raw_buffer_store_b32(v, r, off, 0, 16); }
DEV bool ok4(u32x4 v) { return v[0] != 0xFFFFFFFFu && v[1] != 0xFFFFFFFFu && v[2] != 0xFFFFFFFFu && v[3] != 0xFFFFFFFFu; }
DEV bool allw(bool p) { return __builtin_amdgcn_ballot_w64(!p) == 0; }
DEV void pin4(u32x4& v) { asm volatile("" : "+v"(v)); }
DEV void tr88(const u32x4* r, u32x4* f) {
#pragma unroll
  for (int j = 0; j < 8; ++j) {
#pragma unroll
    for (int p = 0; p < 4; ++p) f[j][p] = __builtin_amdgcn_perm(r[2 * p + 1][j >> 1], r[2 * p][j >> 1], (j & 1) ? 0x07060302u : 0x05040100u);
    pin4(f[j]);
  }
}
DEV void tr84(const u32x2* r, u32x4* f) {
#pragma unroll
  for (int j = 0; j < 4; ++j) {
#pragma unroll
    for (int p = 0; p < 4; ++p) f[j][p] = __builtin_amdgcn_perm(r[2 * p + 1][j >> 1], r[2 * p][j >> 1], (j & 1) ? 0x07060302u : 0x05040100u);
    pin4(f[j]);
  }
}
DEV float sw32add(float a, float b) { auto r = __builtin_amdgcn_permlane32_swap(__float_as_uint(a), __float_as_uint(b), false, false); return __uint_as_float(r[0]) + __uint_as_float(r[1]); }
DEV float sw16add(float a, float b) { auto r = __builtin_amdgcn_permlane16_swap(__float_as_uint(a), __float_as_uint(b), false, false); return __uint_as_float(r[0]) + __uint_as_float(r[1]); }
DEV float sw32max(float a) { auto r = __builtin_amdgcn_permlane32_swap(__float_as_uint(a), __float_as_uint(a), false, false); return fmaxf(__uint_as_float(r[0]), __uint_as_float(r[1])); }
DEV float sw16max(float a) { auto r = __builtin_amdgcn_permlane16_swap(__float_as_uint(a), __float_as_uint(a), false, false); return fmaxf(__uint_as_float(r[0]), __uint_as_float(r[1])); }
template <int N> DEV float ror(float a) { return __uint_as_float(__builtin_amdgcn_update_dpp(0u, __float_as_uint(a), 0x120 + N, 0xf, 0xf, false)); }
DEV float rowsum(float a) { a += ror<8>(a); a += ror<4>(a); a += ror<2>(a); a += ror<1>(a); return a; }
DEV float rowmax(float a) { a = fmaxf(a, ror<8>(a)); a = fmaxf(a, ror<4>(a)); a = fmaxf(a, ror<2>(a)); a = fmaxf(a, ror<1>(a)); return a; }
DEV float wavesum(float a) { a = rowsum(a); a = sw16add(a, a); a = sw32add(a, a); return a; }
DEV void glds16(const void* g, u32 lds) {
  u32 keep;
  asm volatile("s_mov_b32 %0, m0\n\ts_mov_b32 m0, %2\n\ts_nop 0\n\tglobal_load_lds_dwordx4 %1, off\n\ts_mov_b32 m0, %0" : "=&s"(keep) : "v"(g), "s"(lds) : "memory");
}
typedef __attribute__((ext_vector_type(4))) short s16x4;
DEV u32x2 trread(const char* p) {
  s16x4 v = __builtin_amdgcn_ds_read_tr16_b64_v4i16((__attribute__((address_space(3))) s16x4*)(p));
  return __builtin_bit_cast(u32x2, v);
}
DEV void poll_ne(rsrc_t r, u32 off) {
  for (int it = 0; it < SPIN_MAX; ++it) { u32 v = cld32(r, off); fence(); if (__builtin_amdgcn_readfirstlane(v) != 0xFFFFFFFFu) break; __builtin_amdgcn_s_sleep(1); }
}
DEV void poll_eq(rsrc_t r, u32 off, u32 tag) {
  for (int it = 0; it < SPIN_MAX; ++it) { u32 v = cld32(r, off); fence(); if (__builtin_amdgcn_readfirstlane(v) == tag) break; __builtin_amdgcn_s_sleep(1); }
}
DEV float lnmod(float v, float mean, float rs, float sc, float sh) { return rbf(rbf(rbf(rbf(v - mean) / rs) * rbf(1.f + sc)) + sh); }

__global__ __launch_bounds__(512, 2) void mk_kernel(const u16* x, const u16* ckv, const u16* wq, const u16* wuk,
    const u16* wuv, const u16* wo, const u16* wg, const u16* wu, const u16* wd,
    const u16* cc, const u16* wmod, u16* out, char* ws) {
  __shared__ __attribute__((aligned(16))) char lds[L_TOTAL];
  const int tid = threadIdx.x, lane = tid & 63, wave = __builtin_amdgcn_readfirstlane(tid >> 6), bid = blockIdx.x;
  const int g = lane >> 4, c16 = lane & 15;
  const int h = (bid & 7) + 8 * ((bid >> 3) & 3), cs = bid >> 5, hg = (bid >> 4) & 1;
  const int aidx = (bid & 15) + 16 * (bid >> 5), ab = aidx >> 6, aks = aidx & 63;
  const int cb = cs >> 2, cq = cs & 3;
  const rsrc_t rws = mk(ws, W_BYTES);
  const u32x4 SENT = {0xFFFFFFFFu, 0xFFFFFFFFu, 0xFFFFFFFFu, 0xFFFFFFFFu};
  const f32x4 Z4 = {0.f, 0.f, 0.f, 0.f};
  float* red = (float*)(lds + L_RED);
  TS(0);
  const u32 tag = __builtin_amdgcn_readfirstlane(cld32(rws, W_EPOCH)) + 1u;

  if (bid < 48) {
    const int p = (bid & 16 ? 16 : 0) + (bid & 32) + 0;
    const int blk = 2 * (bid & 7) + ((bid >> 3) & 1);
    const int col0 = (p << 6) + 32 * blk;
    const rsrc_t rm = mk(wmod, 512 * 3072 * 2), rc = mk(cc, 2048), rx = mk(x, 2048);
    if (bid < 16 && tid < 256) cst128(rws, W_QB + bid * 4096 + tid * 16, SENT);
    u32x2 r[2][8]; u32x4 cf[2];
#pragma unroll
    for (int t = 0; t < 2; ++t) {
#pragma unroll
      for (int i = 0; i < 8; ++i) r[t][i] = ld64(rm, ((64 * wave + 32 * t + 8 * g + i) * 3072 + col0 + (c16 >> 3) * 512 + 4 * (c16 & 7)) * 2);
      cf[t] = ld128(rc, ((lane & 1) * 512 + 64 * wave + 32 * t + 8 * g) * 2);
    }
    float mean = 0.f, rs = 0.f, xv = 0.f;
    if (bid < 16 && wave == 0) {
      u32x4 x0 = ld128(rx, lane * 16), x1 = ld128(rx, 1024 + lane * 16);
      u32 xx = __builtin_amdgcn_raw_buffer_load_b16(rx, ((lane >> 5) * 512 + 32 * blk + (lane & 31)) * 2, 0, 0);
      float s0 = 0.f, s1 = 0.f;
#pragma unroll
      for (int q = 0; q < 4; ++q) { s0 += bflo(x0[q]) + bfhi(x0[q]); s1 += bflo(x1[q]) + bfhi(x1[q]); }
      s0 = wavesum(s0) * (1.f / 512.f); s1 = wavesum(s1) * (1.f / 512.f);
      float v0 = 0.f, v1 = 0.f;
#pragma unroll
      for (int q = 0; q < 4; ++q) {
        float a = bflo(x0[q]) - s0, b = bfhi(x0[q]) - s0, c = bflo(x1[q]) - s1, d = bfhi(x1[q]) - s1;
        v0 += a * a + b * b; v1 += c * c + d * d;
      }
      v0 = wavesum(v0) * (1.f / 512.f); v1 = wavesum(v1) * (1.f / 512.f);
      mean = rbf(lane < 32 ? s0 : s1); rs = rbf(sqrtf(rbf(rbf(lane < 32 ? v0 : v1) + 1e-5f))); xv = bf2f(xx);
    }
    f32x4 acc[4] = {Z4, Z4, Z4, Z4};
#pragma unroll
    for (int t = 0; t < 2; ++t) {
      u32x4 a;
#pragma unroll
      for (int q = 0; q < 4; ++q) { float lo = bflo(cf[t][q]), hi = bfhi(cf[t][q]); a[q] = pk2(lo / (1.f + __expf(-lo)), hi / (1.f + __expf(-hi))); }
      u32x4 f[4]; tr84(r[t], f);
#pragma unroll
      for (int j = 0; j < 4; ++j) acc[j] = mfma(a, f[j], acc[j]);
    }
    if (lane < 32) { f32x4 v = {acc[0][0], acc[1][0], acc[2][0], acc[3][0]}, w = {acc[0][1], acc[1][1], acc[2][1], acc[3][1]}; *(f32x4*)(red + wave * 128 + g * 64 + 4 * c16) = g ? w : v; }
    waitvm0();
    bar();
    if (wave == 0) {
      float lo = 0.f, hi = 0.f;
#pragma unroll
      for (int w = 0; w < 8; ++w) { lo += red[w * 128 + (lane >> 5) * 64 + (lane & 31)]; hi += red[w * 128 + (lane >> 5) * 64 + 32 + (lane & 31)]; }
      lo = rbf(lo); hi = rbf(hi);
      if (bid < 16) {
        float hv = lnmod(xv, mean, rs, hi, lo);
        float ho = __shfl_xor(hv, 32);
        if (lane < 32) cst64(rws, W_HG + (32 * blk + lane) * 8, u32x2{tag, pk2(hv, ho)});
      } else {
        float lo2 = __shfl_xor(lo, 32), hi2 = __shfl_xor(hi, 32);
        u32 d = lane < 32 ? pk2(lo, lo2) : pk2(hi2, hi);
        cst64(rws, W_MG + (col0 - 1024 + (lane >> 5) * 512 + (lane & 31)) * 8, u32x2{tag, d});
      }
    }
    fence();
  }
  TS(1);
  u32x4 qf[2][8], uk[2][4];
  {
    const rsrc_t rq = mk(wq, 512 * 4096 * 2), rk = mk(wuk, 512 * 4096 * 2);
    u32x4 r[2][8];
#pragma unroll
    for (int t = 0; t < 2; ++t)
#pragma unroll
      for (int i = 0; i < 8; ++i) r[t][i] = ld128(rq, ((64 * wave + 32 * t + 8 * g + i) * 4096 + h * 128 + 8 * c16) * 2);
    if (wave < 2) {
#pragma unroll
      for (int t = 0; t < 2; ++t)
#pragma unroll
        for (int k = 0; k < 4; ++k) uk[t][k] = ld128(rk, ((64 * cs + 32 * wave + 8 * (c16 >> 2) + 4 * t + (c16 & 3)) * 4096 + h * 128 + 32 * k + 8 * g) * 2);
    }
    fence();
    tr88(r[0], qf[0]); tr88(r[1], qf[1]);
  }
  TS(2);
  const u32 ldsb = (u32)(size_t)lds;
  const char* kvsrc = (const char*)ckv + ((size_t)(ab * 8192 + aks * 128) * 1024) + lane * 16;
  if (wave >= 4) {
#pragma unroll
    for (int i = 0; i < 16; ++i) { const int row = 32 * (wave - 4) + i; glds16(kvsrc + row * 1024, ldsb + row * KVS); }
  } else {
#pragma unroll
    for (int i = 0; i < 4; ++i) cst128(rws, W_CI + bid * 16384 + i * 4096 + tid * 16, SENT);
    if (tid < 128) cst128(rws, W_DP + ((cb * 128 + h * 4 + cq) * 512) * 4 + tid * 16, SENT);
    if (tid < 4) cst128(rws, W_LSE + (((ab * 2 + hg) * 64 + aks) * 16) * 4 + tid * 16, SENT);
    if (bid >= 96 && bid < 160 && tid < 8) cst128(rws, W_XE + ((hg * 2 + ((bid - 96) >> 5)) * 512 + 32 * (bid & 15)) * 4 + tid * 16, SENT);
    if (bid >= 160 && bid < 216) cst128(rws, W_FP + (bid - 160) * 4096 + tid * 16, SENT);
    poll_eq(rws, W_HG + (wave * 128 + 32 * (bid & 3)) * 8, tag);
    TS(3);
    u32x4 v;
    for (int it = 0; it < SPIN_MAX; ++it) { v = cld128(rws, W_HG + tid * 16); fence(); if (allw(v[0] == tag && v[2] == tag)) break; }
    ((u32*)(lds + L_HS))[tid] = __builtin_amdgcn_perm(v[3], v[1], 0x05040100u);
    ((u32*)(lds + L_HS))[256 + tid] = __builtin_amdgcn_perm(v[3], v[1], 0x07060302u);
    waitvm0();
  }
  bar();
  TS(4);
  {
    f32x4 acc[8];
#pragma unroll
    for (int j = 0; j < 8; ++j) acc[j] = Z4;
#pragma unroll
    for (int t = 0; t < 2; ++t) {
      u32x4 a = *(const u32x4*)(lds + L_HS + ((lane & 1) * 512 + 64 * wave + 32 * t + 8 * g) * 2);
#pragma unroll
      for (int j = 0; j < 8; ++j) acc[j] = mfma(a, qf[t][j], acc[j]);
    }
    if (lane < 32) {
      f32x4 v0 = {acc[0][0], acc[1][0], acc[2][0], acc[3][0]}, v1 = {acc[4][0], acc[5][0], acc[6][0], acc[7][0]};
      f32x4 w0 = {acc[0][1], acc[1][1], acc[2][1], acc[3][1]}, w1 = {acc[4][1], acc[5][1], acc[6][1], acc[7][1]};
      *(f32x4*)(red + wave * 256 + g * 128 + 8 * c16) = g ? w0 : v0;
      *(f32x4*)(red + wave * 256 + g * 128 + 8 * c16 + 4) = g ? w1 : v1;
    }
  }
  bar();
  if (wave >= 4) {
#pragma unroll
    for (int i = 16; i < 32; ++i) { const int row = 32 * (wave - 4) + i; glds16(kvsrc + row * 1024, ldsb + row * KVS); }
  } else if (wave < 2) {
    f32x4 s = Z4;
#pragma unroll
    for (int w = 0; w < 8; ++w) s += *(const f32x4*)(red + w * 256 + 4 * lane);
    char* qp = lds + L_QP + wave * 512;
    *(u32x2*)(qp + lane * 8) = u32x2{pk2(s[0], s[1]), pk2(s[2], s[3])};
    waitlgkm0();
    f32x4 d0 = Z4, d1 = Z4;
#pragma unroll
    for (int k = 0; k < 4; ++k) {
      u32x4 b = *(const u32x4*)(qp + (lane & 1) * 256 + 64 * k + 16 * g);
      d0 = mfma(uk[0][k], b, d0); d1 = mfma(uk[1][k], b, d1);
    }
    const float sc = 0.08838834764831845f * 1.4426950408889634f;
    u32x4 o = {canon(pk2(d0[0] * sc, d0[1] * sc)), canon(pk2(d0[2] * sc, d0[3] * sc)), canon(pk2(d1[0] * sc, d1[1] * sc)), canon(pk2(d1[2] * sc, d1[3] * sc))};
    if (c16 < 2) cst128(rws, W_QB + ((c16 * 32 + h) * 512 + 64 * cs + 32 * wave + 8 * g) * 2, o);
  }
  TS(5);
  const rsrc_t rv = mk(wuv, 512 * 4096 * 2), ro = mk(wo, 4096 * 512 * 2);
  u32x4 uvr[8]; u32x2 wor[4][8];
  const int ksc = wave & 3, sh = wave >> 2;
#define LOAD_C_WEIGHTS() do { \
    _Pragma("unroll") for (int i = 0; i < 8; ++i) uvr[i] = ld128(rv, ((128 * cq + 32 * ksc + 16 * (i >> 2) + 4 * g + (i & 3)) * 4096 + h * 128 + 8 * c16) * 2); \
    _Pragma("unroll") for (int k = 0; k < 4; ++k) _Pragma("unroll") for (int i = 0; i < 8; ++i) wor[k][i] = ld64(ro, ((h * 128 + 32 * k + 8 * g + i) * 512 + 64 * wave + 4 * c16) * 2); \
    fence(); } while (0)
  f32x4 sa[2];
  u32x4 vf[4][4];
  {
    u32x4 kf[2][16];
    if (wave >= 4) {
      waitvm0();
#pragma unroll
      for (int t = 0; t < 2; ++t)
#pragma unroll
        for (int s = 0; s < 16; ++s) { kf[t][s] = *(const u32x4*)(lds + (32 * (wave - 4) + 16 * t + c16) * KVS + 64 * s + 16 * g); pin4(kf[t][s]); }
    } else {
      const u32 qb = W_QB + (ab * 32 + hg * 16) * 1024;
      poll_ne(rws, qb + ((bid * 5 + wave * 37) & 127) * 128);
      TS(6);
      u32x4 v[4];
      for (int it = 0; it < SPIN_MAX; ++it) {
#pragma unroll
        for (int i = 0; i < 4; ++i) v[i] = cld128(rws, qb + (i * 256 + tid) * 16);
        fence();
        if (allw(ok4(v[0]) && ok4(v[1]) && ok4(v[2]) && ok4(v[3]))) break;
      }
#pragma unroll
      for (int i = 0; i < 4; ++i) *(u32x4*)(lds + L_QS + (i * 4 + wave) * 1040 + lane * 16) = v[i];
      LOAD_C_WEIGHTS();
    }
    TS(7);
    bar();
    if (wave >= 4) {
      f32x4 a0 = Z4, a1 = Z4, b0 = Z4, b1 = Z4;
#pragma unroll
      for (int s = 0; s < 16; s += 2) {
        u32x4 q0 = *(const u32x4*)(lds + L_QS + c16 * 1040 + 64 * s + 16 * g), q1 = *(const u32x4*)(lds + L_QS + c16 * 1040 + 64 * s + 64 + 16 * g);
        a0 = mfma(kf[0][s], q0, a0); b0 = mfma(kf[1][s], q0, b0);
        a1 = mfma(kf[0][s + 1], q1, a1); b1 = mfma(kf[1][s + 1], q1, b1);
      }
      sa[0] = a0 + a1; sa[1] = b0 + b1;
      float m = fmaxf(fmaxf(fmaxf(sa[0][0], sa[0][1]), fmaxf(sa[0][2], sa[0][3])), fmaxf(fmaxf(sa[1][0], sa[1][1]), fmaxf(sa[1][2], sa[1][3])));
      m = sw16max(m); m = sw32max(m);
      if (lane < 16) ((float*)(lds + L_MX))[lane * 4 + wave - 4] = m;
      fence();
      LOAD_C_WEIGHTS();
    }
  }
#define LOAD_VF() do { \
    _Pragma("unroll") for (int t = 0; t < 4; ++t) _Pragma("unroll") for (int k = 0; k < 4; ++k) { \
      const char* p = lds + (32 * k + 4 * g + (c16 >> 2)) * KVS + (64 * wave + 16 * t + 4 * (c16 & 3)) * 2; \
      u32x2 lo = trread(p), hi = trread(p + 16 * KVS); vf[t][k] = u32x4{lo[0], lo[1], hi[0], hi[1]}; pin4(vf[t][k]); } } while (0)
  if (wave < 4) LOAD_VF();
  bar();
  const f32x4 mx4 = *(const f32x4*)(lds + L_MX + c16 * 16);
  const float mloc = fmaxf(fmaxf(mx4[0], mx4[1]), fmaxf(mx4[2], mx4[3]));
  if (wave >= 4) {
    float p[8], l = 0.f;
#pragma unroll
    for (int i = 0; i < 8; ++i) { p[i] = __builtin_amdgcn_exp2f(sa[i >> 2][i & 3] - mloc); l += p[i]; }
    *(u32x4*)(lds + L_RED + (wave - 4) * 1024 + lane * 16) = u32x4{pk2(p[0], p[1]), pk2(p[2], p[3]), pk2(p[4], p[5]), pk2(p[6], p[7])};
    l = sw16add(l, l); l = sw32add(l, l);
    if (lane < 16) ((float*)(lds + L_LS))[lane * 4 + wave - 4] = l;
    fence();
    LOAD_VF();
  }
  bar();
  TS(8);
  {
    f32x4 o[4] = {Z4, Z4, Z4, Z4};
#pragma unroll
    for (int k = 0; k < 4; ++k) {
      u32x4 pf = *(const u32x4*)(lds + L_RED + k * 1024 + lane * 16);
#pragma unroll
      for (int t = 0; t < 4; ++t) o[t] = mfma(vf[t][k], pf, o[t]);
    }
    const f32x4 l4 = *(const f32x4*)(lds + L_LS + c16 * 16);
    const float l = (l4[0] + l4[1]) + (l4[2] + l4[3]), rl = 1.f / l;
    const int hd = hg * 16 + c16, dst = (hd & 7) + 8 * (hd >> 3) + 32 * (ab * 4 + (wave >> 1));
    const u32 off = W_CI + dst * 16384 + aks * 256 + (8 * (wave & 1) + g) * 16;
#pragma unroll
    for (int u = 0; u < 2; ++u) {
      u32x4 v = {canon(pk2(o[2 * u][0] * rl, o[2 * u][1] * rl)), canon(pk2(o[2 * u][2] * rl, o[2 * u][3] * rl)), canon(pk2(o[2 * u + 1][0] * rl, o[2 * u + 1][1] * rl)), canon(pk2(o[2 * u + 1][2] * rl, o[2 * u + 1][3] * rl))};
      cst128(rws, off + 64 * u, v);
    }
    if (wave == 0 && lane < 16) cst32(rws, W_LSE + (((ab * 2 + hg) * 64 + aks) * 16 + lane) * 4, canon(__float_as_uint(mloc + __builtin_amdgcn_logf(l))));
    fence();
  }
  TS(9);
  {
    u32x4 uf[8], of[4][4];
    tr88(uvr, uf);
#pragma unroll
    for (int k = 0; k < 4; ++k) tr84(wor[k], of[k]);
    const u32 ib = W_CI + bid * 16384 + (32 * sh + c16) * 256 + (4 * ksc + g) * 16;
    const u32 lo = W_LSE + (((cb * 2 + hg) * 64 + 32 * sh + (lane & 31)) * 16 + (h & 15)) * 4;
    poll_ne(rws, W_CI + bid * 16384 + (32 * sh + ((bid + 7 * wave) & 31)) * 256 + ksc * 64);
    TS(10);
    u32x4 a0, a1; u32 lb;
    for (int it = 0; it < SPIN_MAX; ++it) {
      a0 = cld128(rws, ib); a1 = cld128(rws, ib + 4096); lb = cld32(rws, lo);
      fence();
      if (allw(ok4(a0) && ok4(a1) && lb != 0xFFFFFFFFu)) break;
    }
    TS(11);
    f32x4 d[2][8];
#pragma unroll
    for (int j = 0; j < 8; ++j) { d[0][j] = mfma(a0, uf[j], Z4); d[1][j] = mfma(a1, uf[j], Z4); }
    const float ls = __uint_as_float(lb);
    const float ms = sw16max(rowmax(ls));
    const float e = __builtin_amdgcn_exp2f(ls - ms);
    float es = rowsum(e); es = sw16add(es, es);
    float* ew = (float*)(lds + L_EW) + wave * 32;
    if (lane < 32) ew[lane] = e;
    if (lane == 0 && ksc == 0) *(f32x2*)(lds + L_ML + sh * 8) = f32x2{ms, es};
    waitlgkm0();
    const f32x4 w0 = *(const f32x4*)(ew + 4 * g), w1 = *(const f32x4*)(ew + 16 + 4 * g);
    float v[8];
#pragma unroll
    for (int j = 0; j < 8; ++j)
      v[j] = (w0[0] * d[0][j][0] + w0[1] * d[0][j][1]) + (w0[2] * d[0][j][2] + w0[3] * d[0][j][3]) + (w1[0] * d[1][j][0] + w1[1] * d[1][j][1]) + (w1[2] * d[1][j][2] + w1[3] * d[1][j][3]);
    float u0 = sw32add(v[0], v[4]), u1 = sw32add(v[1], v[5]), u2 = sw32add(v[2], v[6]), u3 = sw32add(v[3], v[7]);
    float z0 = sw16add(u0, u2), z1 = sw16add(u1, u3);
    u32 hi = pk2(z0, z1), lw = pk2(z0 - bflo(hi), z1 - bfhi(hi));
    *(u32*)(lds + L_CX + (2 * wave) * 272 + (8 * c16 + 2 * g) * 2) = hi;
    *(u32*)(lds + L_CX + (2 * wave + 1) * 272 + (8 * c16 + 2 * g) * 2) = lw;
    bar();
    f32x4 oo[4] = {Z4, Z4, Z4, Z4};
#pragma unroll
    for (int k = 0; k < 4; ++k) {
      u32x4 a = *(const u32x4*)(lds + L_CX + c16 * 272 + 64 * k + 16 * g);
#pragma unroll
      for (int j = 0; j < 4; ++j) oo[j] = mfma(a, of[k][j], oo[j]);
    }
    const f32x4 ml = *(const f32x4*)(lds + L_ML);
    const float M = fmaxf(ml[0], ml[2]), f0 = __builtin_amdgcn_exp2f(ml[0] - M), f1 = __builtin_amdgcn_exp2f(ml[2] - M);
    const float F = (g >> 1 ? f1 : f0) / (f0 * ml[1] + f1 * ml[3]);
    float y[4];
#pragma unroll
    for (int j = 0; j < 4; ++j) y[j] = F * ((oo[j][0] + oo[j][1]) + (oo[j][2] + oo[j][3]));
    float z = sw16add(sw32add(y[0], y[2]), sw32add(y[1], y[3]));
    cst32(rws, W_DP + ((cb * 128 + h * 4 + cq) * 512 + 64 * wave + 4 * c16 + g) * 4, canon(__float_as_uint(z)));
    fence();
  }
  TS(12);
  float* r2 = (float*)lds;
  if (bid >= 96 && bid < 160) {
    const int rb = (bid - 96) >> 5, sl = bid & 15;
    const u32 off = W_DP + ((rb * 128 + 64 * hg + (tid >> 3)) * 512 + 32 * sl + 4 * (tid & 7)) * 4;
    poll_ne(rws, W_DP + ((rb * 128 + 64 * hg + 8 * wave + (bid & 7)) * 512 + 32 * sl) * 4);
    u32x4 v;
    for (int it = 0; it < SPIN_MAX; ++it) { v = cld128(rws, off); fence(); if (allw(ok4(v))) break; }
    float z = sw16add(sw32add(__uint_as_float(v[0]), __uint_as_float(v[2])), sw32add(__uint_as_float(v[1]), __uint_as_float(v[3])));
    z += ror<8>(z);
    if (!(lane & 8)) r2[wave * 32 + 4 * (lane & 7) + g] = z;
    bar();
    if (tid < 32) {
      float s = 0.f;
#pragma unroll
      for (int w = 0; w < 8; ++w) s += r2[w * 32 + tid];
      cst32(rws, W_XE + ((hg * 2 + rb) * 512 + 32 * sl + tid) * 4, canon(__float_as_uint(s)));
    }
    TS(13);
  } else if (bid >= 160 && bid < 216) {
    const int rel = bid - 160, fb = rel < 48 ? 2 * (rel & 7) + ((rel >> 3) & 1) + (rel & 48) : rel;
    const rsrc_t rg = mk(wg, 512 * 1792 * 2), ru = mk(wu, 512 * 1792 * 2), rd = mk(wd, 1792 * 512 * 2), rx = mk(x, 2048);
    const u16* wgu = c16 & 8 ? wu : wg;
    u32x2 r[2][8], dr[8];
#pragma unroll
    for (int t = 0; t < 2; ++t)
#pragma unroll
      for (int i = 0; i < 8; ++i) r[t][i] = *(const u32x2*)(wgu + (64 * wave + 32 * t + 8 * g + i) * 1792 + 32 * fb + 4 * (c16 & 7));
#pragma unroll
    for (int i = 0; i < 8; ++i) dr[i] = ld64(rd, ((32 * fb + 8 * g + i) * 512 + 64 * wave + 4 * c16) * 2);
    u32 xa = __builtin_amdgcn_raw_buffer_load_b16(rx, tid * 2, 0, 0), xb = __builtin_amdgcn_raw_buffer_load_b16(rx, 1024 + tid * 2, 0, 0);
    u32x2 mg[4];
    for (int it = 0; it < SPIN_MAX; ++it) {
#pragma unroll
      for (int i = 0; i < 4; ++i) mg[i] = cld64(rws, W_MG + (i * 512 + tid) * 8);
      fence();
      if (allw(mg[0][0] == tag && mg[1][0] == tag && mg[2][0] == tag && mg[3][0] == tag)) break;
    }
    u32x4 gf[2][4], df[4];
    tr84(r[0], gf[0]); tr84(r[1], gf[1]); tr84(dr, df);
    poll_ne(rws, W_XE + (((wave >> 1) & 3) * 512 + 64 * wave + 32 * (bid & 1)) * 4);
    TS(13);
    u32 e[4];
    for (int it = 0; it < SPIN_MAX; ++it) {
#pragma unroll
      for (int i = 0; i < 4; ++i) e[i] = cld32(rws, W_XE + (i * 512 + tid) * 4);
      fence();
      if (allw(e[0] != 0xFFFFFFFFu && e[1] != 0xFFFFFFFFu && e[2] != 0xFFFFFFFFu && e[3] != 0xFFFFFFFFu)) break;
    }
    const float x10 = rbf(bf2f(xa) + rbf(bflo(mg[0][1]) * rbf(__uint_as_float(e[0]) + __uint_as_float(e[2]))));
    const float x11 = rbf(bf2f(xb) + rbf(bfhi(mg[0][1]) * rbf(__uint_as_float(e[1]) + __uint_as_float(e[3]))));
    {
      const float m0 = wavesum(x10) * (1.f / 64.f), m1 = wavesum(x11) * (1.f / 64.f);
      const float q0 = wavesum((x10 - m0) * (x10 - m0)), q1 = wavesum((x11 - m1) * (x11 - m1));
      if (lane == 0) *(f32x4*)(r2 + wave * 4) = f32x4{m0, q0, m1, q1};
      r2[1024 + tid] = x10; r2[1536 + tid] = x11;
      ((u32*)r2)[2048 + tid] = mg[3][1];
    }
    bar();
    {
      f32x4 st[8]; float m0 = 0.f, m1 = 0.f;
#pragma unroll
      for (int w = 0; w < 8; ++w) { st[w] = *(const f32x4*)(r2 + w * 4); m0 += st[w][0]; m1 += st[w][2]; }
      m0 *= 0.125f; m1 *= 0.125f;
      float q0 = 0.f, q1 = 0.f;
#pragma unroll
      for (int w = 0; w < 8; ++w) { q0 += st[w][1] + 64.f * (st[w][0] - m0) * (st[w][0] - m0); q1 += st[w][3] + 64.f * (st[w][2] - m1) * (st[w][2] - m1); }
      const float rs0 = rbf(sqrtf(rbf(rbf(q0 * (1.f / 512.f)) + 1e-5f))), rs1 = rbf(sqrtf(rbf(rbf(q1 * (1.f / 512.f)) + 1e-5f)));
      const float h0 = lnmod(x10, rbf(m0), rs0, bflo(mg[2][1]), bflo(mg[1][1])), h1 = lnmod(x11, rbf(m1), rs1, bfhi(mg[2][1]), bfhi(mg[1][1]));
      const u32 hh = pk2(h0, h1);
      ((u16*)(lds + L_HS))[tid] = (u16)hh; ((u16*)(lds + L_HS))[512 + tid] = (u16)(hh >> 16);
    }
    bar();
    {
      f32x4 acc[4] = {Z4, Z4, Z4, Z4};
#pragma unroll
      for (int t = 0; t < 2; ++t) {
        u32x4 a = *(const u32x4*)(lds + L_HS + ((lane & 1) * 512 + 64 * wave + 32 * t + 8 * g) * 2);
#pragma unroll
        for (int j = 0; j < 4; ++j) acc[j] = mfma(a, gf[t][j], acc[j]);
      }
      if (lane < 32) { f32x4 v = {acc[0][0], acc[1][0], acc[2][0], acc[3][0]}, w = {acc[0][1], acc[1][1], acc[2][1], acc[3][1]}; *(f32x4*)(red + wave * 128 + g * 64 + 4 * c16) = g ? w : v; }
    }
    bar();
    {
      float gg = 0.f, uu = 0.f;
#pragma unroll
      for (int w = 0; w < 8; ++w) { gg += red[w * 128 + (lane >> 5) * 64 + (lane & 31)]; uu += red[w * 128 + (lane >> 5) * 64 + 32 + (lane & 31)]; }
      gg = rbf(gg); uu = rbf(uu);
      const float zz = 0.7978845608028654f * (gg + 0.044715f * gg * gg * gg);
      const float th = 1.f - 2.f / (__expf(2.f * zz) + 1.f);
      const float act = rbf(0.5f * gg * (1.f + th)) * uu;
      u16* ap = (u16*)(r2 + 3072) + wave * 64;
      ap[lane] = (u16)pk2(act, 0.f);
      waitlgkm0();
      const u32x4 a = *(const u32x4*)(ap + (lane & 1) * 32 + 8 * g);
      f32x4 dd[4];
#pragma unroll
      for (int j = 0; j < 4; ++j) dd[j] = mfma(a, df[j], Z4);
      const int s = g & 1;
      u32x4 v = {canon(__float_as_uint(s ? dd[0][1] : dd[0][0])), canon(__float_as_uint(s ? dd[1][1] : dd[1][0])), canon(__float_as_uint(s ? dd[2][1] : dd[2][0])), canon(__float_as_uint(s ? dd[3][1] : dd[3][0]))};
      if (lane < 32) cst128(rws, W_FP + rel * 4096 + (g * 512 + 64 * wave + 4 * c16) * 4, v);
      fence();
    }
    TS(14);
    if (rel < 32) {
      const int rb = rel >> 4, sl = rel & 15;
      float z = 0.f;
      if (wave < 7) {
        const u32 off = W_FP + (tid >> 3) * 4096 + (rb * 512 + 32 * sl + 4 * (tid & 7)) * 4;
        poll_ne(rws, W_FP + (8 * wave + (bid & 7)) * 4096 + (rb * 512 + 32 * sl) * 4);
        u32x4 v;
        for (int it = 0; it < SPIN_MAX; ++it) { v = cld128(rws, off); fence(); if (allw(ok4(v))) break; }
        z = sw16add(sw32add(__uint_as_float(v[0]), __uint_as_float(v[2])), sw32add(__uint_as_float(v[1]), __uint_as_float(v[3])));
        z += ror<8>(z);
      }
      if (!(lane & 8)) r2[256 + wave * 32 + 4 * (lane & 7) + g] = z;
      bar();
      if (tid < 32) {
        float s = 0.f;
#pragma unroll
        for (int w = 0; w < 8; ++w) s += r2[256 + w * 32 + tid];
        const int m = 32 * sl + tid;
        const u32 gbp = ((const u32*)r2)[2048 + m];
        const float gb = rb ? bfhi(gbp) : bflo(gbp);
        out[rb * 512 + m] = (u16)pk2(r2[1024 + rb * 512 + m] + rbf(gb * rbf(s)), 0.f);
        if (rel == 0 && tid == 0) cst32(rws, W_EPOCH, tag);
      }
      TS(15);
    }
  }
}

extern "C" void kernel_launch(void* const* d_in, const int* in_sizes, int n_in, void* d_out, int out_size, void* d_ws, size_t ws_size, hipStream_t stream) {
  mk_kernel<<<256, 512, 0, stream>>>((const u16*)d_in[0], (const u16*)d_in[1], (const u16*)d_in[2], (const u16*)d_in[3], (const u16*)d_in[4], (const u16*)d_in[5],
      (const u16*)d_in[7], (const u16*)d_in[8], (const u16*)d_in[9], (const u16*)d_in[10], (const u16*)d_in[11], (u16*)d_out, (char*)d_ws);
}
```

```cpp
#include <hip/hip_runtime.h>
typedef unsigned short u16;
typedef unsigned u32;
typedef __attribute__((ext_vector_type(4))) unsigned u32x4;
typedef __attribute__((ext_vector_type(2))) unsigned u32x2;
typedef __attribute__((ext_vector_type(4))) float f32x4;
typedef __attribute__((ext_vector_type(8))) __bf16 bf16x8;
typedef __attribute__((ext_vector_type(2))) __bf16 bf16x2;
typedef __attribute__((ext_vector_type(2))) float f32x2;
#define DEV __device__ __forceinline__

#define W_EPOCH 0
#define W_M1 4096
#define W_M2 16384
#define W_QG 32768
#define W_QB 65536
#define W_XE 131072
#define W_LSI 196608
#define W_FP 262144
#define W_DP 524288
#define W_CI 1048576
#define W_TS (1048576 + 4194304)
#define W_BYTES (W_TS + 256 * 32 * 8)
#define KVS 1056
#define L_P 135168
#define L_MX 143360
#define L_LS 143872
#define L_HS 144384
#define L_RED 146432
#define L_QP 150528
#define L_CX1 152576
#define L_CX2 156928
#define L_TOTAL 161280
#define SPIN_MAX 400000

#define TS(i)

DEV float bf2f(u32 v) { return __uint_as_float(v << 16); }
DEV float bflo(u32 v) { return __uint_as_float(v << 16); }
DEV float bfhi(u32 v) { return __uint_as_float(v & 0xffff0000u); }
DEV u32 pk2(float a, float b) { f32x2 v = {a, b}; bf16x2 r = __builtin_convertvector(v, bf16x2); return __builtin_bit_cast(u32, r); }
DEV float rbf(float a) { return bflo(pk2(a, 0.f)); }
DEV u32 canon(u32 v) { return v < 0xFFFFFFFEu ? v : 0xFFFFFFFEu; }
DEV f32x4 mfma(u32x4 a, u32x4 b, f32x4 c) { return __builtin_amdgcn_mfma_f32_16x16x32_bf16(__builtin_bit_cast(bf16x8, a), __builtin_bit_cast(bf16x8, b), c, 0, 0, 0); }
DEV void fence() { asm volatile("" ::: "memory"); }
DEV void bar() { asm volatile("s_waitcnt lgkmcnt(0)\n\ts_barrier" ::: "memory"); }
DEV void waitvm0() { asm volatile("s_waitcnt vmcnt(0)" ::: "memory"); }
DEV void waitlgkm0() { asm volatile("s_waitcnt lgkmcnt(0)" ::: "memory"); }
typedef __amdgpu_buffer_rsrc_t rsrc_t;
DEV rsrc_t mk(const void* p, u32 bytes) { return __builtin_amdgcn_make_buffer_rsrc((void*)p, 0, bytes, 0x00020000); }
DEV u32x4 ld128(rsrc_t r, u32 off) { return __builtin_amdgcn_raw_buffer_load_b128(r, off, 0, 0); }
DEV u32x2 ld64(rsrc_t r, u32 off) { return __builtin_amdgcn_raw_buffer_load_b64(r, off, 0, 0); }
DEV u32 ld32(rsrc_t r, u32 off) { return __builtin_amdgcn_raw_buffer_load_b32(r, off, 0, 0); }
DEV u32x4 nt128(rsrc_t r, u32 off) { return __builtin_amdgcn_raw_buffer_load_b128(r, off, 0, 2); }
DEV u32x2 nt64(rsrc_t r, u32 off) { return __builtin_amdgcn_raw_buffer_load_b64(r, off, 0, 2); }
DEV float dot2(u32 a, u32 b, float c) { return __builtin_amdgcn_fdot2_f32_bf16(__builtin_bit_cast(bf16x2, a), __builtin_bit_cast(bf16x2, b), c, false); }
DEV u32x4 cld128(rsrc_t r, u32 off) { return __builtin_amdgcn_raw_buffer_load_b128(r, off, 0, 16); }
DEV u32x2 cld64(rsrc_t r, u32 off) { return __builtin_amdgcn_raw_buffer_load_b64(r, off, 0, 16); }
DEV u32 cld32(rsrc_t r, u32 off) { return __builtin_amdgcn_raw_buffer_load_b32(r, off, 0, 16); }
DEV void cst128(rsrc_t r, u32 off, u32x4 v) { __builtin_amdgcn_raw_buffer_store_b128(v, r, off, 0, 16); }
DEV void cst64(rsrc_t r, u32 off, u32x2 v) { __builtin_amdgcn_raw_buffer_store_b64(v, r, off, 0, 16); }
DEV void cst32(rsrc_t r, u32 off, u32 v) { __builtin_amdgcn_raw_buffer_store_b32(v, r, off, 0, 16); }
DEV bool ok4(u32x4 v) { return v[0] != 0xFFFFFFFFu && v[1] != 0xFFFFFFFFu && v[2] != 0xFFFFFFFFu && v[3] != 0xFFFFFFFFu; }
DEV bool allw(bool p) { return __builtin_amdgcn_ballot_w64(!p) == 0; }
DEV void pin4(u32x4& v) { asm volatile("" : "+v"(v)); }
DEV void tr88(const u32x4* r, u32x4* f) {
#pragma unroll
  for (int j = 0; j < 8; ++j) {
#pragma unroll
    for (int p = 0; p < 4; ++p) f[j][p] = __builtin_amdgcn_perm(r[2 * p + 1][j >> 1], r[2 * p][j >> 1], (j & 1) ? 0x07060302u : 0x05040100u);
    pin4(f[j]);
  }
}
DEV void tr84(const u32x2* r, u32x4* f) {
#pragma unroll
  for (int j = 0; j < 4; ++j) {
#pragma unroll
    for (int p = 0; p < 4; ++p) f[j][p] = __builtin_amdgcn_perm(r[2 * p + 1][j >> 1], r[2 * p][j >> 1], (j & 1) ? 0x07060302u : 0x05040100u);
    pin4(f[j]);
  }
}
DEV float sw32add(float a, float b) { auto r = __builtin_amdgcn_permlane32_swap(__float_as_uint(a), __float_as_uint(b), false, false); return __uint_as_float(r[0]) + __uint_as_float(r[1]); }
DEV float sw16add(float a, float b) { auto r = __builtin_amdgcn_permlane16_swap(__float_as_uint(a), __float_as_uint(b), false, false); return __uint_as_float(r[0]) + __uint_as_float(r[1]); }
DEV float sw32max(float a) { auto r = __builtin_amdgcn_permlane32_swap(__float_as_uint(a), __float_as_uint(a), false, false); return fmaxf(__uint_as_float(r[0]), __uint_as_float(r[1])); }
DEV float sw16max(float a) { auto r = __builtin_amdgcn_permlane16_swap(__float_as_uint(a), __float_as_uint(a), false, false); return fmaxf(__uint_as_float(r[0]), __uint_as_float(r[1])); }
template <int N> DEV float ror(float a) { return __uint_as_float(__builtin_amdgcn_update_dpp(0u, __float_as_uint(a), 0x120 + N, 0xf, 0xf, false)); }
DEV float rowsum(float a) { a += ror<8>(a); a += ror<4>(a); a += ror<2>(a); a += ror<1>(a); return a; }
DEV float rowmax(float a) { a = fmaxf(a, ror<8>(a)); a = fmaxf(a, ror<4>(a)); a = fmaxf(a, ror<2>(a)); a = fmaxf(a, ror<1>(a)); return a; }
DEV float wavesum(float a) { a = rowsum(a); a = sw16add(a, a); a = sw32add(a, a); return a; }
DEV void glds16(const void* g, u32 lds) {
  u32 keep;
  asm volatile("s_mov_b32 %0, m0\n\ts_mov_b32 m0, %2\n\ts_nop 0\n\tglobal_load_lds_dwordx4 %1, off nt\n\ts_mov_b32 m0, %0" : "=&s"(keep) : "v"(g), "s"(lds) : "memory");
}
typedef __attribute__((ext_vector_type(4))) short s16x4;
DEV u32x2 trread(const char* p) {
  s16x4 v = __builtin_amdgcn_ds_read_tr16_b64_v4i16((__attribute__((address_space(3))) s16x4*)(p));
  return __builtin_bit_cast(u32x2, v);
}
DEV void poll_ne(rsrc_t r, u32 off) {
  for (int it = 0; it < SPIN_MAX; ++it) { u32 v = cld32(r, off); fence(); if (__builtin_amdgcn_readfirstlane(v) != 0xFFFFFFFFu) break; __builtin_amdgcn_s_sleep(1); }
}
DEV void poll_eq(rsrc_t r, u32 off, u32 tag) {
  for (int it = 0; it < SPIN_MAX; ++it) { u32 v = cld32(r, off); fence(); if (allw(v == tag)) break; __builtin_amdgcn_s_sleep(1); }
}
DEV float lnmod(float v, float mean, float rs, float sc, float sh) { return rbf(rbf(rbf(rbf(v - mean) / rs) * rbf(1.f + sc)) + sh); }

__global__ __launch_bounds__(512, 2) void mk_kernel(const u16* x, const u16* ckv, const u16* wq, const u16* wuk,
    const u16* wuv, const u16* wo, const u16* wg, const u16* wu, const u16* wd,
    const u16* cc, const u16* wmod, u16* out, char* ws) {
  __shared__ __attribute__((aligned(16))) char lds[L_TOTAL];
  const int tid = threadIdx.x, lane = tid & 63, wave = __builtin_amdgcn_readfirstlane(tid >> 6), bid = blockIdx.x;
  const int g = lane >> 4, c16 = lane & 15;
  const bool isF = bid < 128;
  const int h = (bid & 7) + 8 * ((bid >> 3) & 3), cq = (bid >> 5) & 3;
  const int an = bid - 128, ab = (an >> 6) & 1, aks = an & 63;
  const rsrc_t rws = mk(ws, W_BYTES);
  const u32x4 SENT = {0xFFFFFFFFu, 0xFFFFFFFFu, 0xFFFFFFFFu, 0xFFFFFFFFu};
  const f32x4 Z4 = {0.f, 0.f, 0.f, 0.f};
  float* red = (float*)(lds + L_RED);
  const u32 ldsb = (u32)(size_t)lds;
  TS(0);
  const u32 tag = cld32(rws, W_EPOCH) + 1u;
  fence();
  const bool modw = isF ? wave == 0 : wave < 2;
  u32x4 mr[8], mc[2];
  int mcol = 0;
  if (modw) {
    const int line = (bid & 7) + 8 * (isF ? (bid >> 6) : 2 + ((an >> 5) & 3));
    mcol = 64 * line + (isF ? 8 * ((bid >> 3) & 7) : 16 * ((bid >> 3) & 3) + 8 * wave);
    const rsrc_t rm = mk(wmod, 512 * 3072 * 2), rc = mk(cc, 2048);
    if (isF && lane < 32) cst128(rws, W_QB + (((lane >> 4) * 32 + h) * 512 + 128 * cq) * 2 + c16 * 16, SENT);
#pragma unroll
    for (int i = 0; i < 8; ++i) mr[i] = ld128(rm, ((8 * lane + i) * 3072 + mcol) * 2);
    mc[0] = ld128(rc, lane * 16); mc[1] = ld128(rc, 1024 + lane * 16);
    fence();
  }
  if (!isF) {
    const char* kvsrc = (const char*)ckv + ((size_t)(ab * 8192 + aks * 128 + 16 * wave) * 1024) + lane * 16;
#pragma unroll
    for (int i = 0; i < 16; ++i) glds16(kvsrc + i * 1024, ldsb + (16 * wave + i) * KVS);
    if (an >= 64 && an < 120 && tid < 256) cst128(rws, W_FP + (an - 64) * 4096 + tid * 16, SENT);
    TS(1);
  }
  if (modw) {
    float v[2][8];
    u32x4 f[8]; tr88(mr, f);
#pragma unroll
    for (int b = 0; b < 2; ++b) {
      u32x4 a;
#pragma unroll
      for (int q = 0; q < 4; ++q) { float lo = bflo(mc[b][q]), hi = bfhi(mc[b][q]); a[q] = pk2(lo / (1.f + __expf(-lo)), hi / (1.f + __expf(-hi))); }
#pragma unroll
      for (int j = 0; j < 8; ++j) v[b][j] = dot2(f[j][3], a[3], dot2(f[j][2], a[2], dot2(f[j][1], a[1], dot2(f[j][0], a[0], 0.f))));
    }
    float z[2][2];
#pragma unroll
    for (int b = 0; b < 2; ++b) {
      float u0 = sw32add(v[b][0], v[b][4]), u1 = sw32add(v[b][1], v[b][5]), u2 = sw32add(v[b][2], v[b][6]), u3 = sw32add(v[b][3], v[b][7]);
      z[b][0] = rowsum(sw16add(u0, u2)); z[b][1] = rowsum(sw16add(u1, u3));
    }
    const u32 d = c16 & 1 ? pk2(z[0][1], z[1][1]) : pk2(z[0][0], z[1][0]);
    const int col = mcol + 2 * g + c16;
    const u32 off = isF ? W_M1 + ((col & 511) * 2 + (col >> 9)) * 8 : W_M2 + (col - 1024) * 8;
    if (isF) waitvm0();
    if (c16 < 2) cst64(rws, off, u32x2{tag, d});
    fence();
    if (isF) TS(1);
  }
  if (isF) {
    u32x4 qf[8], uk[2][4];
    const int wq4 = wave & 3;
    if (wave >= 4) {
      const rsrc_t rq = mk(wq, 512 * 4096 * 2), rk = mk(wuk, 512 * 4096 * 2);
      u32x4 r[8];
#pragma unroll
      for (int i = 0; i < 8; ++i) r[i] = ld128(rq, ((128 * wq4 + 32 * (c16 >> 2) + 8 * g + i) * 4096 + h * 128 + 32 * cq + 8 * (c16 & 3)) * 2);
#pragma unroll
      for (int t = 0; t < 2; ++t)
#pragma unroll
        for (int k = 0; k < 4; ++k) uk[t][k] = nt128(rk, ((128 * cq + 32 * wq4 + 8 * (c16 >> 2) + 4 * t + (c16 & 3)) * 4096 + h * 128 + 32 * k + 8 * g) * 2);
      fence();
      TS(1);
      tr88(r, qf);
#pragma unroll
      for (int t = 0; t < 2; ++t)
#pragma unroll
        for (int k = 0; k < 4; ++k) pin4(uk[t][k]);
      TS(2);
    } else {
      const rsrc_t rx = mk(x, 2048);
      u32x4 x0 = ld128(rx, lane * 16), x1 = ld128(rx, 1024 + lane * 16);
      const u32 xa = ld32(rx, tid * 4), xb = ld32(rx, 1024 + tid * 4);
      if (wave == 3) {
        if (lane < 32) cst128(rws, W_LSI + bid * 512 + lane * 16, SENT);
#pragma unroll
        for (int i = 0; i < 4; ++i) cst128(rws, W_DP + (((i >> 1) * 128 + bid) * 512) * 4 + (i & 1) * 1024 + lane * 16, SENT);
      } else if (wave) {
#pragma unroll
        for (int i = 0; i < 16; ++i) cst128(rws, W_CI + bid * 32768 + (wave - 1) * 16384 + i * 1024 + lane * 16, SENT);
      }
      fence();
      float s0 = 0.f, s1 = 0.f;
#pragma unroll
      for (int q = 0; q < 4; ++q) { s0 += bflo(x0[q]) + bfhi(x0[q]); s1 += bflo(x1[q]) + bfhi(x1[q]); }
      s0 = wavesum(s0) * (1.f / 512.f); s1 = wavesum(s1) * (1.f / 512.f);
      float v0 = 0.f, v1 = 0.f;
#pragma unroll
      for (int q = 0; q < 4; ++q) {
        float a = bflo(x0[q]) - s0, b = bfhi(x0[q]) - s0, c = bflo(x1[q]) - s1, d = bfhi(x1[q]) - s1;
        v0 += a * a + b * b; v1 += c * c + d * d;
      }
      v0 = wavesum(v0) * (1.f / 512.f); v1 = wavesum(v1) * (1.f / 512.f);
      const float r0 = rbf(sqrtf(rbf(rbf(v0) + 1e-5f))), r1 = rbf(sqrtf(rbf(rbf(v1) + 1e-5f)));
      s0 = rbf(s0); s1 = rbf(s1);
      const float n00 = rbf(rbf(bflo(xa) - s0) / r0), n01 = rbf(rbf(bfhi(xa) - s0) / r0), n10 = rbf(rbf(bflo(xb) - s1) / r1), n11 = rbf(rbf(bfhi(xb) - s1) / r1);
      TS(2);
      poll_eq(rws, W_M1 + (128 * wave + 8 * ((bid >> 3) & 15)) * 16 + (bid & 1) * 8, tag);
      TS(3);
      u32x4 v, w;
      for (int it = 0; it < SPIN_MAX; ++it) {
        v = cld128(rws, W_M1 + tid * 32); w = cld128(rws, W_M1 + tid * 32 + 16);
        fence();
        if (allw(v[0] == tag && v[2] == tag && w[0] == tag && w[2] == tag)) break;
      }
      ((u32*)(lds + L_HS))[tid] = pk2(rbf(n00 * rbf(1.f + bflo(v[3]))) + bflo(v[1]), rbf(n01 * rbf(1.f + bflo(w[3]))) + bflo(w[1]));
      ((u32*)(lds + L_HS))[256 + tid] = pk2(rbf(n10 * rbf(1.f + bfhi(v[3]))) + bfhi(v[1]), rbf(n11 * rbf(1.f + bfhi(w[3]))) + bfhi(w[1]));
      waitvm0();
    }
    bar();
    TS(4);
    if (wave >= 4) {
      const u32x4 a = *(const u32x4*)(lds + L_HS + ((c16 & 1) * 512 + 128 * wq4 + 32 * ((c16 >> 1) & 3) + 8 * g) * 2);
      f32x4 acc[8];
#pragma unroll
      for (int j = 0; j < 8; ++j) acc[j] = mfma(a, qf[j], Z4);
      const bool sel = c16 & 4;
      f32x4 o[4];
#pragma unroll
      for (int j = 0; j < 8; ++j) { o[j >> 2][j & 3] = sel ? acc[j][2] : acc[j][0]; o[2 + (j >> 2)][j & 3] = sel ? acc[j][3] : acc[j][1]; }
      if (g == (c16 >> 3)) {
        float* p = red + (wq4 * 4 + (c16 >> 2)) * 64 + 8 * (c16 & 3);
        *(f32x4*)p = o[0]; *(f32x4*)(p + 4) = o[1]; *(f32x4*)(p + 32) = o[2]; *(f32x4*)(p + 36) = o[3];
      }
    }
    bar();
    if (wave >= 4) {
      if (wave == 4) {
        float s = 0.f;
#pragma unroll
        for (int w = 0; w < 16; ++w) s += red[w * 64 + lane];
        const float so = __shfl_xor(s, 32);
        if (lane < 32) cst64(rws, W_QG + (h * 128 + 32 * cq + lane) * 8, u32x2{tag, pk2(s, so)});
        fence();
      }
      TS(5);
      u32x4 v;
      for (int it = 0; it < SPIN_MAX; ++it) { v = cld128(rws, W_QG + h * 1024 + lane * 16); fence(); if (allw(v[0] == tag && v[2] == tag)) break; }
      TS(6);
      char* qp = lds + L_QP + wq4 * 512;
      *(u32*)(qp + lane * 4) = __builtin_amdgcn_perm(v[3], v[1], 0x05040100u);
      *(u32*)(qp + 256 + lane * 4) = __builtin_amdgcn_perm(v[3], v[1], 0x07060302u);
      waitlgkm0();
      f32x4 d0 = Z4, d1 = Z4;
#pragma unroll
      for (int k = 0; k < 4; ++k) {
        u32x4 b = *(const u32x4*)(qp + (lane & 1) * 256 + 64 * k + 16 * g);
        d0 = mfma(uk[0][k], b, d0); d1 = mfma(uk[1][k], b, d1);
      }
      const float sc = 0.08838834764831845f * 1.4426950408889634f;
      u32x4 o = {canon(pk2(d0[0] * sc, d0[1] * sc)), canon(pk2(d0[2] * sc, d0[3] * sc)), canon(pk2(d1[0] * sc, d1[1] * sc)), canon(pk2(d1[2] * sc, d1[3] * sc))};
      if (c16 < 2) cst128(rws, W_QB + ((c16 * 32 + h) * 512 + 128 * cq + 32 * wq4 + 8 * g) * 2, o);
      fence();
    }
    bar();
    TS(7);
    const int dh = wave >> 2;
    u32x4 uf[4], of[4][4];
    {
      const rsrc_t rv = mk(wuv, 512 * 4096 * 2), ro = mk(wo, 4096 * 512 * 2);
      u32x2 uvr[8], wor[4][8];
#pragma unroll
      for (int i = 0; i < 8; ++i) uvr[i] = nt64(rv, ((128 * cq + 32 * wq4 + 16 * (i >> 2) + 4 * g + (i & 3)) * 4096 + h * 128 + 64 * dh + 4 * c16) * 2);
#pragma unroll
      for (int k = 0; k < 4; ++k)
#pragma unroll
        for (int i = 0; i < 8; ++i) wor[k][i] = ld64(ro, ((h * 128 + 32 * k + 8 * g + i) * 512 + 64 * wave + 4 * c16) * 2);
      fence();
      TS(8);
      tr84(uvr, uf);
#pragma unroll
      for (int k = 0; k < 4; ++k) tr84(wor[k], of[k]);
    }
    TS(9);
    const u32 ib = W_CI + bid * 32768 + ((dh * 64 + 16 * wq4 + 4 * g) * 16 + c16) * 16;
    const u32 lb = W_LSI + bid * 512 + dh * 256;
    poll_ne(rws, W_CI + bid * 32768 + (dh * 64 + 16 * wq4 + (bid & 15)) * 256);
    TS(10);
    u32x4 a[4], l4; u32 l1;
    for (int it = 0; it < SPIN_MAX; ++it) {
#pragma unroll
      for (int i = 0; i < 4; ++i) a[i] = cld128(rws, ib + i * 256);
      l4 = cld128(rws, lb + (16 * wq4 + 4 * g) * 4); l1 = cld32(rws, lb + lane * 4);
      fence();
      if (allw(ok4(a[0]) && ok4(a[1]) && ok4(a[2]) && ok4(a[3]) && l1 != 0xFFFFFFFFu && ok4(l4))) break;
    }
    TS(11);
    {
      const float ls = __uint_as_float(l1);
      const float ms = sw32max(sw16max(rowmax(ls)));
      const float rl = 1.f / wavesum(__builtin_amdgcn_exp2f(ls - ms));
      float acc[8];
#pragma unroll
      for (int i = 0; i < 4; ++i) {
        const float w = __builtin_amdgcn_exp2f(__uint_as_float(l4[i]) - ms);
#pragma unroll
        for (int q = 0; q < 4; ++q) {
          acc[2 * q] = i ? fmaf(w, bflo(a[i][q]), acc[2 * q]) : w * bflo(a[i][q]);
          acc[2 * q + 1] = i ? fmaf(w, bfhi(a[i][q]), acc[2 * q + 1]) : w * bfhi(a[i][q]);
        }
      }
      float u0 = sw32add(acc[0], acc[4]), u1 = sw32add(acc[1], acc[5]), u2 = sw32add(acc[2], acc[6]), u3 = sw32add(acc[3], acc[7]);
      const float z0 = sw16add(u0, u2) * rl, z1 = sw16add(u1, u3) * rl;
      const u32 hi = pk2(z0, z1), lw = pk2(z0 - bflo(hi), z1 - bfhi(hi));
      *(u32*)(lds + L_CX1 + (2 * wave) * 272 + (8 * c16 + 2 * g) * 2) = hi;
      *(u32*)(lds + L_CX1 + (2 * wave + 1) * 272 + (8 * c16 + 2 * g) * 2) = lw;
    }
    bar();
    {
      const u32x4 aa = *(const u32x4*)(lds + L_CX1 + c16 * 272 + 64 * wq4 + 16 * g);
      float y[4];
#pragma unroll
      for (int j = 0; j < 4; ++j) { f32x4 d = mfma(aa, uf[j], Z4); y[j] = (d[0] + d[1]) + (d[2] + d[3]); }
      const float z0 = sw16add(y[0], y[2]), z1 = sw16add(y[1], y[3]);
      const u32 hi = pk2(z0, z1), lw = pk2(z0 - bflo(hi), z1 - bfhi(hi));
      char* p = lds + L_CX2 + (8 * (g >> 1) + 2 * wq4) * 272 + (64 * dh + 4 * c16 + 2 * (g & 1)) * 2;
      *(u32*)p = hi; *(u32*)(p + 272) = lw;
    }
    bar();
    {
      f32x4 oo[4] = {Z4, Z4, Z4, Z4};
#pragma unroll
      for (int k = 0; k < 4; ++k) {
        u32x4 aa = *(const u32x4*)(lds + L_CX2 + c16 * 272 + 64 * k + 16 * g);
#pragma unroll
        for (int j = 0; j < 4; ++j) oo[j] = mfma(aa, of[k][j], oo[j]);
      }
      float y[4];
#pragma unroll
      for (int j = 0; j < 4; ++j) y[j] = (oo[j][0] + oo[j][1]) + (oo[j][2] + oo[j][3]);
      const float z0 = sw16add(y[0], y[2]), z1 = sw16add(y[1], y[3]);
      cst64(rws, W_DP + (((g >> 1) * 128 + bid) * 512 + 64 * wave + 4 * c16 + 2 * (g & 1)) * 4, u32x2{canon(__float_as_uint(z0)), canon(__float_as_uint(z1))});
    }
    TS(12);
    return;
  }
  {
    const int kq = wave & 3, hh = wave >> 2;
    waitvm0();
    TS(2);
    bar();
    u32x4 kf[2][16], vf[4][4];
#pragma unroll
    for (int t = 0; t < 2; ++t)
#pragma unroll
      for (int s = 0; s < 16; ++s) { kf[t][s] = *(const u32x4*)(lds + (32 * kq + 16 * t + c16) * KVS + 64 * s + 16 * g); pin4(kf[t][s]); }
#pragma unroll
    for (int t = 0; t < 4; ++t)
#pragma unroll
      for (int k = 0; k < 4; ++k) {
        const char* p = lds + (32 * k + 4 * g + (c16 >> 2)) * KVS + (64 * wave + 16 * t + 4 * (c16 & 3)) * 2;
        u32x2 lo = trread(p), hi = trread(p + 16 * KVS); vf[t][k] = u32x4{lo[0], lo[1], hi[0], hi[1]}; pin4(vf[t][k]);
      }
    if (wave == 2) {
      for (int it = 0; it < SPIN_MAX; ++it) { u32x4 v = cld128(rws, W_M1 + lane * 128); fence(); if (allw(v[0] == tag && v[2] == tag)) break; __builtin_amdgcn_s_sleep(2); }
    }
    bar();
    TS(3);
    {
      const u32 qb = W_QB + (ab * 32 + 4 * wave) * 1024;
      poll_ne(rws, qb + (an & 15) * 256);
      TS(4);
      u32x4 v[4];
      for (int it = 0; it < SPIN_MAX; ++it) {
#pragma unroll
        for (int i = 0; i < 4; ++i) v[i] = cld128(rws, qb + i * 1024 + lane * 16);
        fence();
        if (allw(ok4(v[0]) && ok4(v[1]) && ok4(v[2]) && ok4(v[3]))) break;
      }
#pragma unroll
      for (int i = 0; i < 4; ++i) *(u32x4*)(lds + (4 * wave + i) * 1040 + lane * 16) = v[i];
    }
    TS(5);
    bar();
    f32x4 a0 = Z4, a1 = Z4, b0 = Z4, b1 = Z4;
#pragma unroll
    for (int s = 0; s < 16; s += 2) {
      u32x4 q0 = *(const u32x4*)(lds + (16 * hh + c16) * 1040 + 64 * s + 16 * g), q1 = *(const u32x4*)(lds + (16 * hh + c16) * 1040 + 64 * s + 64 + 16 * g);
      a0 = mfma(kf[0][s], q0, a0); b0 = mfma(kf[1][s], q0, b0);
      a1 = mfma(kf[0][s + 1], q1, a1); b1 = mfma(kf[1][s + 1], q1, b1);
    }
    a0 += a1; b0 += b1;
    float m = fmaxf(fmaxf(fmaxf(a0[0], a0[1]), fmaxf(a0[2], a0[3])), fmaxf(fmaxf(b0[0], b0[1]), fmaxf(b0[2], b0[3])));
    m = sw16max(m); m = sw32max(m);
    if (lane < 16) ((float*)(lds + L_MX))[(16 * hh + lane) * 4 + kq] = m;
    bar();
    TS(6);
    {
      const f32x4 mx4 = *(const f32x4*)(lds + L_MX + (16 * hh + c16) * 16);
      const float ml = fmaxf(fmaxf(mx4[0], mx4[1]), fmaxf(mx4[2], mx4[3]));
      float p[8], l = 0.f;
#pragma unroll
      for (int i = 0; i < 8; ++i) { p[i] = __builtin_amdgcn_exp2f((i < 4 ? a0[i & 3] : b0[i & 3]) - ml); l += p[i]; }
      *(u32x4*)(lds + L_P + wave * 1024 + lane * 16) = u32x4{pk2(p[0], p[1]), pk2(p[2], p[3]), pk2(p[4], p[5]), pk2(p[6], p[7])};
      l = sw16add(l, l); l = sw32add(l, l);
      if (lane < 16) ((float*)(lds + L_LS))[(16 * hh + lane) * 4 + kq] = l;
    }
    bar();
    TS(7);
#pragma unroll
    for (int t2 = 0; t2 < 2; ++t2) {
      f32x4 o[4] = {Z4, Z4, Z4, Z4};
#pragma unroll
      for (int k = 0; k < 4; ++k) {
        u32x4 pf = *(const u32x4*)(lds + L_P + (4 * t2 + k) * 1024 + lane * 16);
#pragma unroll
        for (int t = 0; t < 4; ++t) o[t] = mfma(vf[t][k], pf, o[t]);
      }
      const int hd = 16 * t2 + c16;
      const f32x4 l4 = *(const f32x4*)(lds + L_LS + hd * 16);
      const float rl = 1.f / ((l4[0] + l4[1]) + (l4[2] + l4[3]));
      const u32 off = W_CI + (hd + 32 * (wave >> 1)) * 32768 + (ab * 64 + aks) * 256 + (8 * (wave & 1) + g) * 16;
#pragma unroll
      for (int u = 0; u < 2; ++u) {
        u32x4 v = {canon(pk2(o[2 * u][0] * rl, o[2 * u][1] * rl)), canon(pk2(o[2 * u][2] * rl, o[2 * u][3] * rl)), canon(pk2(o[2 * u + 1][0] * rl, o[2 * u + 1][1] * rl)), canon(pk2(o[2 * u + 1][2] * rl, o[2 * u + 1][3] * rl))};
        cst128(rws, off + 64 * u, v);
      }
    }
    if (wave < 2) {
      const int hd = lane & 31;
      const f32x4 l4 = *(const f32x4*)(lds + L_LS + hd * 16), mx4 = *(const f32x4*)(lds + L_MX + hd * 16);
      const float lse = fmaxf(fmaxf(mx4[0], mx4[1]), fmaxf(mx4[2], mx4[3])) + __builtin_amdgcn_logf((l4[0] + l4[1]) + (l4[2] + l4[3]));
      cst32(rws, W_LSI + (tid * 128 + ab * 64 + aks) * 4, canon(__float_as_uint(lse)));
    }
    fence();
    TS(8);
  }
  float* r2 = (float*)lds;
  if (an < 64) {
    const int rb = an >> 5, sl = an & 31;
    const u32 off = W_DP + ((rb * 128 + (tid >> 2)) * 512 + 16 * sl + 4 * (tid & 3)) * 4;
    poll_ne(rws, W_DP + ((rb * 128 + 16 * wave + (an & 15)) * 512 + 16 * sl) * 4);
    TS(9);
    u32x4 v;
    for (int it = 0; it < SPIN_MAX; ++it) { v = cld128(rws, off); fence(); if (allw(ok4(v))) break; }
    float z = sw16add(sw32add(__uint_as_float(v[0]), __uint_as_float(v[2])), sw32add(__uint_as_float(v[1]), __uint_as_float(v[3])));
    z += ror<8>(z); z += ror<4>(z);
    if (c16 < 4) r2[wave * 16 + 4 * c16 + g] = z;
    bar();
    if (tid < 16) {
      float s = 0.f;
#pragma unroll
      for (int w = 0; w < 8; ++w) s += r2[w * 16 + tid];
      cst64(rws, W_XE + ((16 * sl + tid) * 2 + rb) * 8, u32x2{tag, __float_as_uint(s)});
    }
    TS(10);
  } else if (an < 120) {
    const int rel = an - 64, fb = rel < 48 ? 2 * (rel & 7) + ((rel >> 3) & 1) + (rel & 48) : rel;
    const rsrc_t rd = mk(wd, 1792 * 512 * 2), rx = mk(x, 2048);
    const u16* wgu = c16 & 8 ? wu : wg;
    u32x2 r[2][8], dr[8];
#pragma unroll
    for (int t = 0; t < 2; ++t)
#pragma unroll
      for (int i = 0; i < 8; ++i) r[t][i] = *(const u32x2*)(wgu + (64 * wave + 32 * t + 8 * g + i) * 1792 + 32 * fb + 4 * (c16 & 7));
#pragma unroll
    for (int i = 0; i < 8; ++i) dr[i] = nt64(rd, ((32 * fb + 8 * g + i) * 512 + 64 * wave + 4 * c16) * 2);
    u32 xa = __builtin_amdgcn_raw_buffer_load_b16(rx, tid * 2, 0, 0), xb = __builtin_amdgcn_raw_buffer_load_b16(rx, 1024 + tid * 2, 0, 0);
    u32x2 mg[4];
    for (int it = 0; it < SPIN_MAX; ++it) {
#pragma unroll
      for (int i = 0; i < 4; ++i) mg[i] = cld64(rws, W_M2 + (i * 512 + tid) * 8);
      fence();
      if (allw(mg[0][0] == tag && mg[1][0] == tag && mg[2][0] == tag && mg[3][0] == tag)) break;
    }
    u32x4 gf[2][4], df[4];
    tr84(r[0], gf[0]); tr84(r[1], gf[1]); tr84(dr, df);
    TS(9);
    poll_eq(rws, W_XE + (64 * wave + 16 * (an & 3)) * 16 + ((an >> 2) & 1) * 8, tag);
    TS(10);
    u32x4 e;
    for (int it = 0; it < SPIN_MAX; ++it) { e = cld128(rws, W_XE + tid * 16); fence(); if (allw(e[0] == tag && e[2] == tag)) break; }
    const float x10 = rbf(bf2f(xa) + rbf(bflo(mg[0][1]) * rbf(__uint_as_float(e[1]))));
    const float x11 = rbf(bf2f(xb) + rbf(bfhi(mg[0][1]) * rbf(__uint_as_float(e[3]))));
    {
      const float m0 = wavesum(x10) * (1.f / 64.f), m1 = wavesum(x11) * (1.f / 64.f);
      const float q0 = wavesum((x10 - m0) * (x10 - m0)), q1 = wavesum((x11 - m1) * (x11 - m1));
      if (lane == 0) *(f32x4*)(r2 + wave * 4) = f32x4{m0, q0, m1, q1};
      r2[1024 + tid] = x10; r2[1536 + tid] = x11;
      ((u32*)r2)[2048 + tid] = mg[3][1];
    }
    bar();
    {
      f32x4 st[8]; float m0 = 0.f, m1 = 0.f;
#pragma unroll
      for (int w = 0; w < 8; ++w) { st[w] = *(const f32x4*)(r2 + w * 4); m0 += st[w][0]; m1 += st[w][2]; }
      m0 *= 0.125f; m1 *= 0.125f;
      float q0 = 0.f, q1 = 0.f;
#pragma unroll
      for (int w = 0; w < 8; ++w) { q0 += st[w][1] + 64.f * (st[w][0] - m0) * (st[w][0] - m0); q1 += st[w][3] + 64.f * (st[w][2] - m1) * (st[w][2] - m1); }
      const float rs0 = rbf(sqrtf(rbf(rbf(q0 * (1.f / 512.f)) + 1e-5f))), rs1 = rbf(sqrtf(rbf(rbf(q1 * (1.f / 512.f)) + 1e-5f)));
      const float h0 = lnmod(x10, rbf(m0), rs0, bflo(mg[2][1]), bflo(mg[1][1])), h1 = lnmod(x11, rbf(m1), rs1, bfhi(mg[2][1]), bfhi(mg[1][1]));
      const u32 hh = pk2(h0, h1);
      ((u16*)(lds + L_HS))[tid] = (u16)hh; ((u16*)(lds + L_HS))[512 + tid] = (u16)(hh >> 16);
    }
    bar();
    {
      f32x4 acc[4] = {Z4, Z4, Z4, Z4};
#pragma unroll
      for (int t = 0; t < 2; ++t) {
        u32x4 a = *(const u32x4*)(lds + L_HS + ((lane & 1) * 512 + 64 * wave + 32 * t + 8 * g) * 2);
#pragma unroll
        for (int j = 0; j < 4; ++j) acc[j] = mfma(a, gf[t][j], acc[j]);
      }
      if (lane < 32) { f32x4 v = {acc[0][0], acc[1][0], acc[2][0], acc[3][0]}, w = {acc[0][1], acc[1][1], acc[2][1], acc[3][1]}; *(f32x4*)(red + wave * 128 + g * 64 + 4 * c16) = g ? w : v; }
    }
    bar();
    {
      float gg = 0.f, uu = 0.f;
#pragma unroll
      for (int w = 0; w < 8; ++w) { gg += red[w * 128 + (lane >> 5) * 64 + (lane & 31)]; uu += red[w * 128 + (lane >> 5) * 64 + 32 + (lane & 31)]; }
      gg = rbf(gg); uu = rbf(uu);
      const float zz = 0.7978845608028654f * (gg + 0.044715f * gg * gg * gg);
      const float th = 1.f - 2.f / (__expf(2.f * zz) + 1.f);
      const float act = rbf(0.5f * gg * (1.f + th)) * uu;
      u16* ap = (u16*)(r2 + 3072) + wave * 64;
      ap[lane] = (u16)pk2(act, 0.f);
      waitlgkm0();
      const u32x4 a = *(const u32x4*)(ap + (lane & 1) * 32 + 8 * g);
      f32x4 dd[4];
#pragma unroll
      for (int j = 0; j < 4; ++j) dd[j] = mfma(a, df[j], Z4);
      const int s = g & 1;
      u32x4 v = {canon(__float_as_uint(s ? dd[0][1] : dd[0][0])), canon(__float_as_uint(s ? dd[1][1] : dd[1][0])), canon(__float_as_uint(s ? dd[2][1] : dd[2][0])), canon(__float_as_uint(s ? dd[3][1] : dd[3][0]))};
      if (lane < 32) cst128(rws, W_FP + rel * 4096 + (g * 512 + 64 * wave + 4 * c16) * 4, v);
      fence();
    }
    TS(11);
    if (rel < 32) {
      const int rb = rel >> 4, sl = rel & 15;
      float z = 0.f;
      if (wave < 7) {
        const u32 off = W_FP + (tid >> 3) * 4096 + (rb * 512 + 32 * sl + 4 * (tid & 7)) * 4;
        poll_ne(rws, W_FP + (8 * wave + (bid & 7)) * 4096 + (rb * 512 + 32 * sl) * 4);
        u32x4 v;
        for (int it = 0; it < SPIN_MAX; ++it) { v = cld128(rws, off); fence(); if (allw(ok4(v))) break; }
        z = sw16add(sw32add(__uint_as_float(v[0]), __uint_as_float(v[2])), sw32add(__uint_as_float(v[1]), __uint_as_float(v[3])));
        z += ror<8>(z);
      }
      if (!(lane & 8)) r2[256 + wave * 32 + 4 * (lane & 7) + g] = z;
      bar();
      if (tid < 32) {
        float s = 0.f;
#pragma unroll
        for (int w = 0; w < 8; ++w) s += r2[256 + w * 32 + tid];
        const int m = 32 * sl + tid;
        const u32 gbp = ((const u32*)r2)[2048 + m];
        const float gb = rb ? bfhi(gbp) : bflo(gbp);
        out[rb * 512 + m] = (u16)pk2(r2[1024 + rb * 512 + m] + rbf(gb * rbf(s)), 0.f);
        if (rel == 0 && tid == 0) cst32(rws, W_EPOCH, tag);
      }
      TS(12);
    }
  }
}

extern "C" void kernel_launch(void* const* d_in, const int* in_sizes, int n_in, void* d_out, int out_size, void* d_ws, size_t ws_size, hipStream_t stream) {
  mk_kernel<<<256, 512, 0, stream>>>((const u16*)d_in[0], (const u16*)d_in[1], (const u16*)d_in[2], (const u16*)d_in[3], (const u16*)d_in[4], (const u16*)d_in[5],
      (const u16*)d_in[7], (const u16*)d_in[8], (const u16*)d_in[9], (const u16*)d_in[10], (const u16*)d_in[11], (u16*)d_out, (char*)d_ws);
}
```

```cpp
#include <hip/hip_runtime.h>
typedef unsigned short u16;
typedef unsigned u32;
typedef __attribute__((ext_vector_type(4))) unsigned u32x4;
typedef __attribute__((ext_vector_type(2))) unsigned u32x2;
typedef __attribute__((ext_vector_type(4))) float f32x4;
typedef __attribute__((ext_vector_type(8))) __bf16 bf16x8;
typedef __attribute__((ext_vector_type(2))) __bf16 bf16x2;
typedef __attribute__((ext_vector_type(2))) float f32x2;
#define DEV __device__ __forceinline__

#define W_EPOCH 0
#define W_GATE 1024
#define W_GATE2 2048
#define W_M2 16384
#define W_M1 32768
#define W_QB 98304
#define W_XE 163840
#define W_LSI 196608
#define W_QG 5308416
#define W_FP 262144
#define W_DP 524288
#define W_CI 1048576
#define W_TS (1048576 + 4194304)
#define W_VG (5308416 + 262144)
#define W_BYTES (5308416 + 524288)
#define KVS 1056
#define L_P 135168
#define L_MX 143360
#define L_LS 143872
#define L_HS 144384
#define L_RED 146432
#define L_QP 150528
#define L_CX1 152576
#define L_CX2 156928
#define L_TOTAL 161280
#define SPIN_MAX 400000
#define NOPD
#define BWD 0
#define MRD 0
#define FFD 0
#define KVD 0
#define CIAUX 16
#define CLRPOS 2
#define FSL 4
#define FSL2 24

#define TS(i)

DEV float bf2f(u32 v) { return __uint_as_float(v << 16); }
DEV float bflo(u32 v) { return __uint_as_float(v << 16); }
DEV float bfhi(u32 v) { return __uint_as_float(v & 0xffff0000u); }
DEV u32 pk2(float a, float b) { f32x2 v = {a, b}; bf16x2 r = __builtin_convertvector(v, bf16x2); return __builtin_bit_cast(u32, r); }
DEV float rbf(float a) { return bflo(pk2(a, 0.f)); }
DEV u32 canon(u32 v) { return v < 0xFFFFFFFEu ? v : 0xFFFFFFFEu; }
DEV f32x4 mfma(u32x4 a, u32x4 b, f32x4 c) { return __builtin_amdgcn_mfma_f32_16x16x32_bf16(__builtin_bit_cast(bf16x8, a), __builtin_bit_cast(bf16x8, b), c, 0, 0, 0); }
DEV void fence() { asm volatile("" ::: "memory"); }
DEV void bar() { asm volatile("s_waitcnt lgkmcnt(0)\n\ts_barrier" ::: "memory"); }
DEV void waitvm0() { asm volatile("s_waitcnt vmcnt(0)" ::: "memory"); }
DEV void waitlgkm0() { asm volatile("s_waitcnt lgkmcnt(0)" ::: "memory"); }
typedef __amdgpu_buffer_rsrc_t rsrc_t;
DEV rsrc_t mk(const void* p, u32 bytes) { return __builtin_amdgcn_make_buffer_rsrc((void*)p, 0, bytes, 0x00020000); }
DEV u32x4 ld128(rsrc_t r, u32 off) { return __builtin_amdgcn_raw_buffer_load_b128(r, off, 0, 0); }
DEV u32x2 ld64(rsrc_t r, u32 off) { return __builtin_amdgcn_raw_buffer_load_b64(r, off, 0, 0); }
DEV u32 ld32(rsrc_t r, u32 off) { return __builtin_amdgcn_raw_buffer_load_b32(r, off, 0, 0); }
DEV u32x4 nt128(rsrc_t r, u32 off) { return __builtin_amdgcn_raw_buffer_load_b128(r, off, 0, 2); }
DEV u32x2 nt64(rsrc_t r, u32 off) { return __builtin_amdgcn_raw_buffer_load_b64(r, off, 0, 2); }
DEV float dot2(u32 a, u32 b, float c) { return __builtin_amdgcn_fdot2_f32_bf16(__builtin_bit_cast(bf16x2, a), __builtin_bit_cast(bf16x2, b), c, false); }
DEV u32x4 cld128(rsrc_t r, u32 off) { return __builtin_amdgcn_raw_buffer_load_b128(r, off, 0, 16); }
DEV u32x2 cld64(rsrc_t r, u32 off) { return __builtin_amdgcn_raw_buffer_load_b64(r, off, 0, 16); }
DEV u32 cld32(rsrc_t r, u32 off) { return __builtin_amdgcn_raw_buffer_load_b32(r, off, 0, 16); }
DEV void cst128(rsrc_t r, u32 off, u32x4 v) { __builtin_amdgcn_raw_buffer_store_b128(v, r, off, 0, 16); }
DEV void cst64(rsrc_t r, u32 off, u32x2 v) { __builtin_amdgcn_raw_buffer_store_b64(v, r, off, 0, 16); }
DEV void cst32(rsrc_t r, u32 off, u32 v) { __builtin_amdgcn_raw_buffer_store_b32(v, r, off, 0, 16); }
DEV bool ok4(u32x4 v) { return v[0] != 0xFFFFFFFFu && v[1] != 0xFFFFFFFFu && v[2] != 0xFFFFFFFFu && v[3] != 0xFFFFFFFFu; }
DEV bool allw(bool p) { return __builtin_amdgcn_ballot_w64(!p) == 0; }
DEV void pin4(u32x4& v) { asm volatile("" : "+v"(v)); }
DEV void tr88(const u32x4* r, u32x4* f) {
#pragma unroll
  for (int j = 0; j < 8; ++j) {
#pragma unroll
    for (int p = 0; p < 4; ++p) f[j][p] = __builtin_amdgcn_perm(r[2 * p + 1][j >> 1], r[2 * p][j >> 1], (j & 1) ? 0x07060302u : 0x05040100u);
    pin4(f[j]);
  }
}
DEV void tr84(const u32x2* r, u32x4* f) {
#pragma unroll
  for (int j = 0; j < 4; ++j) {
#pragma unroll
    for (int p = 0; p < 4; ++p) f[j][p] = __builtin_amdgcn_perm(r[2 * p + 1][j >> 1], r[2 * p][j >> 1], (j & 1) ? 0x07060302u : 0x05040100u);
    pin4(f[j]);
  }
}
DEV float sw32add(float a, float b) { auto r = __builtin_amdgcn_permlane32_swap(__float_as_uint(a), __float_as_uint(b), false, false); return __uint_as_float(r[0]) + __uint_as_float(r[1]); }
DEV float sw16add(float a, float b) { auto r = __builtin_amdgcn_permlane16_swap(__float_as_uint(a), __float_as_uint(b), false, false); return __uint_as_float(r[0]) + __uint_as_float(r[1]); }
DEV float sw32max(float a) { auto r = __builtin_amdgcn_permlane32_swap(__float_as_uint(a), __float_as_uint(a), false, false); return fmaxf(__uint_as_float(r[0]), __uint_as_float(r[1])); }
DEV float sw16max(float a) { auto r = __builtin_amdgcn_permlane16_swap(__float_as_uint(a), __float_as_uint(a), false, false); return fmaxf(__uint_as_float(r[0]), __uint_as_float(r[1])); }
template <int N> DEV float ror(float a) { return __uint_as_float(__builtin_amdgcn_update_dpp(0u, __float_as_uint(a), 0x120 + N, 0xf, 0xf, false)); }
DEV float rowsum(float a) { a += ror<8>(a); a += ror<4>(a); a += ror<2>(a); a += ror<1>(a); return a; }
DEV float rowmax(float a) { a = fmaxf(a, ror<8>(a)); a = fmaxf(a, ror<4>(a)); a = fmaxf(a, ror<2>(a)); a = fmaxf(a, ror<1>(a)); return a; }
DEV float wavesum(float a) { a = rowsum(a); a = sw16add(a, a); a = sw32add(a, a); return a; }
DEV void glds16(const void* g, u32 lds) {
  u32 keep;
  asm volatile("s_mov_b32 %0, m0\n\ts_mov_b32 m0, %2\n\ts_nop 0\n\tglobal_load_lds_dwordx4 %1, off nt\n\ts_mov_b32 m0, %0" : "=&s"(keep) : "v"(g), "s"(lds) : "memory");
}
typedef __attribute__((ext_vector_type(4))) short s16x4;
DEV u32x2 trread(const char* p) {
  s16x4 v = __builtin_amdgcn_ds_read_tr16_b64_v4i16((__attribute__((address_space(3))) s16x4*)(p));
  return __builtin_bit_cast(u32x2, v);
}
DEV void poll_ne(rsrc_t r, u32 off) {
  _Pragma("nounroll") for (int it = 0; it < SPIN_MAX; ++it) { u32 v = cld32(r, off); fence(); if (__builtin_amdgcn_readfirstlane(v) != 0xFFFFFFFFu) break; __builtin_amdgcn_s_sleep(1); }
}
DEV void poll_eq(rsrc_t r, u32 off, u32 tag) {
  _Pragma("nounroll") for (int it = 0; it < SPIN_MAX; ++it) { u32 v = cld32(r, off); fence(); if (allw(v == tag)) break; __builtin_amdgcn_s_sleep(1); }
}
DEV u32 mktag(u32 e) { u32 t; asm volatile("v_add_u32 %0, 1, %1" : "=v"(t) : "v"(e) : "memory"); return t; }
DEV float rcp(float a) { return __builtin_amdgcn_rcpf(a); }
DEV void pinf(float& v) { asm volatile("" : "+v"(v)); }
DEV float silu(float a) { return rbf(a * rcp(1.f + __expf(-a))); }
DEV void modred(float (*v)[8], float* p, int lane) {
  float u[8]; f32x4 z;
#pragma unroll
  for (int j = 0; j < 8; ++j) u[j] = sw32add(v[0][j], v[1][j]);
#pragma unroll
  for (int j = 0; j < 4; ++j) { float t = sw16add(u[j], u[j + 4]); t += ror<8>(t); t += ror<4>(t); z[j] = t; }
  if (!(lane & 12)) *(f32x4*)(p + (lane >> 5) * 32 + 8 * (lane & 3) + 4 * ((lane >> 4) & 1)) = z;
}
DEV float lnmod(float v, float mean, float rs, float sc, float sh) { return rbf(rbf(rbf(rbf(v - mean) / rs) * rbf(1.f + sc)) + sh); }

__global__ __launch_bounds__(512, 2) void mk_kernel(const u16* x, const u16* ckv, const u16* wq, const u16* wuk,
    const u16* wuv, const u16* wo, const u16* wg, const u16* wu, const u16* wd,
    const u16* cc, const u16* wmod, u16* out, char* ws) {
  __shared__ __attribute__((aligned(16))) char lds[L_TOTAL];
  const int tid = threadIdx.x, lane = tid & 63, wave = __builtin_amdgcn_readfirstlane(tid >> 6), bid = blockIdx.x;
  const int g = lane >> 4, c16 = lane & 15;
  const bool isF = bid < 128;
  const int h = (bid & 7) + 8 * ((bid >> 3) & 3), cq = (bid >> 5) & 3;
  const int an = bid - 128, ab = (an >> 6) & 1, aks = an & 63;
  const rsrc_t rws = mk(ws, W_BYTES);
  const u32x4 SENT = {0xFFFFFFFFu, 0xFFFFFFFFu, 0xFFFFFFFFu, 0xFFFFFFFFu};
  const f32x4 Z4 = {0.f, 0.f, 0.f, 0.f};
  float* red = (float*)(lds + L_RED);
  const u32 ldsb = (u32)(size_t)lds;
  unsigned long long pc; u32 clen, pfv = 0;
  asm volatile("s_getpc_b64 %0\n.Lmk_cs:\n\ts_mov_b32 %1, .Lmk_ce-.Lmk_cs\n\ts_max_u32 %1, %1, .Lmk_cf-.Lmk_cs\n\ts_max_u32 %1, %1, .Lmk_cg-.Lmk_cs" : "=s"(pc), "=s"(clen) :: "scc");
  if (wave == 7) pfv = ld32(mk((const void*)pc, clen), (((bid >> 3) & 3) * 64 + lane) * 128);
  fence();
  asm volatile("" :: "s"(x), "s"(ckv), "s"(wq), "s"(wuk), "s"(cc), "s"(wmod), "s"(ws));
  TS(0);
  const u32 ep = cld32(rws, W_EPOCH);
  u32 tag;
  fence();
  const rsrc_t rm = mk(wmod, 512 * 3072 * 2), rc = mk(cc, 2048);
  if (!isF) {
#define CLR() _Pragma("unroll") \
    for (int i = 0; i < 4; ++i) __builtin_amdgcn_raw_buffer_store_b128(SENT, rws, W_CI + (16 * wave + 4 * i + g) * 32768 + an * 256 + c16 * 16, 0, CIAUX); \
    fence();
    if (an >= 64 && an < 120 && tid < 256) cst128(rws, W_FP + (an - 64) * 4096 + tid * 16, SENT);
    fence();
    __builtin_amdgcn_s_sleep(KVD);
    const char* kvsrc = (const char*)ckv + ((size_t)(ab * 8192 + aks * 128 + 16 * wave) * 1024) + lane * 16;
#pragma unroll
    for (int i = 0; i < 16; ++i) glds16(kvsrc + i * 1024, ldsb + (16 * wave + i) * KVS);
    CLR()
    TS(1);
  }
  if (isF) {
    const int wq4 = wave & 3;
    const int mp = bid & 3, cbk = 2 * (((bid >> 2) & 1) + 2 * ((bid >> 4) & 1)) + ((bid >> 3) & 1);
    if (wave >= 4) {
      cst128(rws, W_DP + (((wq4 >> 1) * 128 + bid) * 512) * 4 + (wq4 & 1) * 1024 + lane * 16, SENT);
      fence();
      __builtin_amdgcn_s_barrier();
      fence();
      const char* ks = (const char*)wuk + ((size_t)(128 * cq + 32 * wq4 + g) * 4096 + h * 128) * 2;
#pragma unroll
      for (int i = 0; i < 8; ++i) glds16(ks + i * 32768 + ((c16 ^ (((i >> 1) << 2) | g)) * 16), ldsb + wq4 * 8192 + i * 1024);
      tag = mktag(ep);
      TS(2);
    } else {
      u32x4 qf[8], mr[8], mc[2];
      if (wave == 0) {
        const int row = 128 * mp + 8 * (lane >> 2);
        if (lane < 32) cst128(rws, W_QB + (((lane >> 4) * 32 + h) * 512 + 128 * cq) * 2 + c16 * 16, SENT);
        if (cq == 0) cst128(rws, W_LSI + h * 1024 + lane * 16, SENT);
        fence();
#pragma unroll
        for (int i = 0; i < 8; ++i) mr[i] = ld128(rm, ((row + i) * 3072 + (cbk >> 2) * 512 + 128 * cq + 32 * (cbk & 3) + 8 * (lane & 3)) * 2);
        mc[0] = ld128(rc, row * 2); mc[1] = ld128(rc, 1024 + row * 2);
        fence();
      } else __builtin_amdgcn_s_sleep(FSL);
      const rsrc_t rx = mk(x, 2048), rq = mk(wq, 512 * 4096 * 2);
      const u32x4 x0 = ld128(rx, lane * 16), x1 = ld128(rx, 1024 + lane * 16);
      const u32 xa = __builtin_amdgcn_raw_buffer_load_b16(rx, (lane >> 5) * 1024 + (128 * cq + 32 * wave + (lane & 31)) * 2, 0, 0);
#pragma unroll
      for (int i = 0; i < 8; ++i) qf[i] = nt128(rq, ((128 * cq + 32 * wave + 8 * g + i) * 4096 + h * 128 + 8 * c16) * 2);
      fence();
      tag = mktag(ep);
      if (wave == 0) {
        u32x4 f[8]; float z[2][2];
        TS(13);
        tr88(mr, f);
#pragma unroll
        for (int b = 0; b < 2; ++b) {
          u32x4 a; float v[8];
#pragma unroll
          for (int q = 0; q < 4; ++q) { float lo = bflo(mc[b][q]), hi = bfhi(mc[b][q]); a[q] = pk2(lo * rcp(1.f + __expf(-lo)), hi * rcp(1.f + __expf(-hi))); }
#pragma unroll
          for (int j = 0; j < 8; ++j) v[j] = dot2(f[j][3], a[3], dot2(f[j][2], a[2], dot2(f[j][1], a[1], dot2(f[j][0], a[0], 0.f))));
          float u0 = sw32add(v[0], v[4]), u1 = sw32add(v[1], v[5]), u2 = sw32add(v[2], v[6]), u3 = sw32add(v[3], v[7]);
          float t0 = sw16add(u0, u2), t1 = sw16add(u1, u3);
          t0 += ror<8>(t0); t1 += ror<8>(t1); z[b][0] = t0 + ror<4>(t0); z[b][1] = t1 + ror<4>(t1);
        }
        const int jj = (lane >> 3) & 1;
        if (!(lane & 4)) cst128(rws, W_M1 + ((cq * 4 + (cbk & 3)) * 4 + mp) * 1024 + (cbk >> 2) * 512 + (8 * (lane & 3) + 2 * g + jj) * 16,
                                u32x4{tag, __float_as_uint(jj ? z[0][1] : z[0][0]), __float_as_uint(jj ? z[1][1] : z[1][0]), tag});
        if (lane == 0) cst32(rws, W_GATE + bid * 4, tag);
        fence();
        TS(1);
      }
      __builtin_amdgcn_s_barrier();
      fence();
      float s0 = 0.f, s1 = 0.f;
#pragma unroll
      for (int q = 0; q < 4; ++q) { s0 += bflo(x0[q]) + bfhi(x0[q]); s1 += bflo(x1[q]) + bfhi(x1[q]); }
      s0 = wavesum(s0) * (1.f / 512.f); s1 = wavesum(s1) * (1.f / 512.f);
      float v0 = 0.f, v1 = 0.f;
#pragma unroll
      for (int q = 0; q < 4; ++q) {
        float a = bflo(x0[q]) - s0, b = bfhi(x0[q]) - s0, c = bflo(x1[q]) - s1, d = bfhi(x1[q]) - s1;
        v0 += a * a + b * b; v1 += c * c + d * d;
      }
      v0 = wavesum(v0) * (1.f / 512.f); v1 = wavesum(v1) * (1.f / 512.f);
      const float rs = rbf(sqrtf(rbf(rbf(lane < 32 ? v0 : v1) + 1e-5f)));
      const float nn = rbf(rbf(bf2f(xa) - rbf(lane < 32 ? s0 : s1)) / rs);
      TS(2);
      u32x4 f[8];
      tr88(qf, f);
      waitvm0();
      TS(3);
      u32x4 v[4];
      _Pragma("nounroll") for (int it = 0; it < SPIN_MAX; ++it) {
#pragma unroll
        for (int i = 0; i < 4; ++i) v[i] = cld128(rws, W_M1 + ((cq * 4 + wave) * 4 + i) * 1024 + lane * 16);
        fence();
        if (allw(v[0][0] == tag && v[1][0] == tag && v[2][0] == tag && v[3][0] == tag)) break;
      }
      TS(4);
      const u32 mm = pk2((__uint_as_float(v[0][1]) + __uint_as_float(v[1][1])) + (__uint_as_float(v[2][1]) + __uint_as_float(v[3][1])),
                         (__uint_as_float(v[0][2]) + __uint_as_float(v[1][2])) + (__uint_as_float(v[2][2]) + __uint_as_float(v[3][2])));
      auto r = __builtin_amdgcn_permlane32_swap(mm, mm, false, false);
      const float sa = lane < 32 ? bflo(r[0]) : bfhi(r[0]), ca = lane < 32 ? bflo(r[1]) : bfhi(r[1]);
      ((u16*)(lds + L_HS))[wave * 64 + lane] = (u16)pk2(rbf(nn * rbf(1.f + ca)) + sa, 0.f);
      waitlgkm0();
      const u32x4 a = *(const u32x4*)(lds + L_HS + wave * 128 + (lane & 1) * 64 + 16 * g);
      f32x4 acc[8];
#pragma unroll
      for (int j = 0; j < 8; ++j) acc[j] = mfma(a, f[j], Z4);
      if (lane < 16) {
        float* p = (float*)(lds + 65536) + wave * 256 + 8 * c16;
        *(f32x4*)p = f32x4{acc[0][0], acc[1][0], acc[2][0], acc[3][0]}; *(f32x4*)(p + 4) = f32x4{acc[4][0], acc[5][0], acc[6][0], acc[7][0]};
        *(f32x4*)(p + 128) = f32x4{acc[0][1], acc[1][1], acc[2][1], acc[3][1]}; *(f32x4*)(p + 132) = f32x4{acc[4][1], acc[5][1], acc[6][1], acc[7][1]};
      }
    }
    bar();
    if (wave < 2) {
      const float* p = (const float*)(lds + 65536) + tid;
      const float q0 = (p[0] + p[256]) + (p[512] + p[768]), q1 = (p[128] + p[384]) + (p[640] + p[896]);
      cst128(rws, W_QG + ((h * 4 + (tid >> 5)) * 4 + cq) * 512 + (lane & 31) * 16, u32x4{tag, __float_as_uint(q0), __float_as_uint(q1), tag});
      fence();
    }
    TS(5);
    u32x4 uk[2][4];
    if (wave >= 4) {
      waitvm0();
#pragma unroll
      for (int t = 0; t < 2; ++t)
#pragma unroll
        for (int k = 0; k < 4; ++k) { uk[t][k] = *(const u32x4*)(lds + wq4 * 8192 + (8 * (c16 >> 2) + 4 * t + (c16 & 3)) * 256 + (((4 * k + g) ^ c16) * 16)); pin4(uk[t][k]); }
      TS(3);
    } else {
      u32x4 v, w;
      _Pragma("nounroll") for (int it = 0; it < SPIN_MAX; ++it) {
        v = cld128(rws, W_QG + (h * 4 + wave) * 2048 + lane * 16); w = cld128(rws, W_QG + (h * 4 + wave) * 2048 + 1024 + lane * 16);
        fence();
        if (allw(v[0] == tag && w[0] == tag)) break;
      }
      TS(6);
      const float z = sw32add(__uint_as_float(v[1]) + __uint_as_float(w[1]), __uint_as_float(v[2]) + __uint_as_float(w[2]));
      ((u16*)(lds + L_QP))[(lane >> 5) * 128 + 32 * wave + (lane & 31)] = (u16)pk2(z, 0.f);
    }
    bar();
    if (wave >= 4) {
      f32x4 d0 = Z4, d1 = Z4;
#pragma unroll
      for (int k = 0; k < 4; ++k) {
        u32x4 b = *(const u32x4*)(lds + L_QP + (lane & 1) * 256 + 64 * k + 16 * g);
        d0 = mfma(uk[0][k], b, d0); d1 = mfma(uk[1][k], b, d1);
      }
      const float sc = 0.08838834764831845f * 1.4426950408889634f;
      u32x4 o = {canon(pk2(d0[0] * sc, d0[1] * sc)), canon(pk2(d0[2] * sc, d0[3] * sc)), canon(pk2(d1[0] * sc, d1[1] * sc)), canon(pk2(d1[2] * sc, d1[3] * sc))};
      if (c16 < 2) cst128(rws, W_QB + ((c16 * 32 + h) * 512 + 128 * cq + 32 * wq4 + 8 * g) * 2, o);
      fence();
    }
    bar();
    TS(7);
    __builtin_amdgcn_s_sleep(BWD);
    u32x2 g2 = cld64(rws, W_GATE2 + lane * 8);
    fence();
    asm volatile("" :: "v"(pfv));
    const int dh = wave >> 2;
    u32x4 uf[4], of[4][4];
    {
      const rsrc_t rv = mk(wuv, 512 * 4096 * 2), ro = mk(wo, 4096 * 512 * 2);
      u32x2 uvr[8], wor[4][8];
#pragma unroll
      for (int i = 0; i < 8; ++i) uvr[i] = nt64(rv, ((128 * cq + 64 * (wq4 >> 1) + 16 * (i & 3) + 8 * (wq4 & 1) + 2 * g + (i >> 2)) * 4096 + h * 128 + 64 * dh + 4 * c16) * 2);
#pragma unroll
      for (int k = 0; k < 4; ++k)
#pragma unroll
        for (int i = 0; i < 8; ++i) wor[k][i] = ld64(ro, ((h * 128 + 32 * ((k + cq) & 3) + 8 * g + i) * 512 + 64 * wave + 4 * c16) * 2);
      fence();
      TS(8);
      tr84(uvr, uf);
#pragma unroll
      for (int k = 0; k < 4; ++k) tr84(wor[k], of[k]);
    }
    _Pragma("nounroll") for (int it = 0; it < SPIN_MAX; ++it) { if (allw(g2[0] == tag && g2[1] == tag)) break; g2 = cld64(rws, W_GATE2 + lane * 8); fence(); }
    TS(9);
    const u32 ib = W_CI + bid * 32768 + ((dh * 64 + 16 * wq4 + 4 * g) * 16 + c16) * 16;
    const u32 lb = W_LSI + h * 1024 + dh * 512;
    poll_ne(rws, W_CI + bid * 32768 + (dh * 64 + 16 * wq4 + (bid & 15)) * 256);
    TS(10);
    u32x4 a[4], l4, l5; u32x2 l1;
    _Pragma("nounroll") for (int it = 0; it < SPIN_MAX; ++it) {
#pragma unroll
      for (int i = 0; i < 4; ++i) a[i] = cld128(rws, ib + i * 256);
      l4 = cld128(rws, lb + (16 * wq4 + 4 * g) * 8); l5 = cld128(rws, lb + (16 * wq4 + 4 * g) * 8 + 16); l1 = cld64(rws, lb + lane * 8);
      fence();
      if (allw(ok4(a[0]) && ok4(a[1]) && ok4(a[2]) && ok4(a[3]) && l1[0] != 0xFFFFFFFFu && l1[1] != 0xFFFFFFFFu && ok4(l4) && ok4(l5))) break;
    }
    TS(11);
    {
      const float ls = __uint_as_float(l1[0]);
      const float ms = sw32max(sw16max(rowmax(ls)));
      const float rl = rcp(wavesum(__builtin_amdgcn_exp2f(ls - ms) * __uint_as_float(l1[1])));
      float acc[8];
#pragma unroll
      for (int i = 0; i < 4; ++i) {
        const float w = __builtin_amdgcn_exp2f(__uint_as_float(i < 2 ? l4[2 * i] : l5[2 * i - 4]) - ms);
#pragma unroll
        for (int q = 0; q < 4; ++q) {
          acc[2 * q] = i ? fmaf(w, bflo(a[i][q]), acc[2 * q]) : w * bflo(a[i][q]);
          acc[2 * q + 1] = i ? fmaf(w, bfhi(a[i][q]), acc[2 * q + 1]) : w * bfhi(a[i][q]);
        }
      }
      float u0 = sw32add(acc[0], acc[4]), u1 = sw32add(acc[1], acc[5]), u2 = sw32add(acc[2], acc[6]), u3 = sw32add(acc[3], acc[7]);
      const float z0 = sw16add(u0, u2) * rl, z1 = sw16add(u1, u3) * rl;
      const u32 hi = pk2(z0, z1), lw = pk2(z0 - bflo(hi), z1 - bfhi(hi));
      *(u32*)(lds + L_CX1 + (2 * wave) * 272 + (8 * c16 + 2 * g) * 2) = hi;
      *(u32*)(lds + L_CX1 + (2 * wave + 1) * 272 + (8 * c16 + 2 * g) * 2) = lw;
    }
    bar();
    {
      const u32x4 aa = *(const u32x4*)(lds + L_CX1 + c16 * 272 + 64 * wq4 + 16 * g);
      float y[4];
#pragma unroll
      for (int j = 0; j < 4; ++j) { f32x4 d = mfma(aa, uf[j], Z4); y[j] = (d[0] + d[1]) + (d[2] + d[3]); }
      const float z0 = sw16add(y[0], y[2]), z1 = sw16add(y[1], y[3]);
      const u32 hi = pk2(z0, z1), lw = pk2(z0 - bflo(hi), z1 - bfhi(hi));
      char* p = lds + L_CX2 + (8 * (g >> 1) + 2 * wq4) * 272 + (64 * dh + 4 * c16 + 2 * (g & 1)) * 2;
      *(u32*)p = hi; *(u32*)(p + 272) = lw;
    }
    bar();
    {
      f32x4 oo[4] = {Z4, Z4, Z4, Z4};
#pragma unroll
      for (int k = 0; k < 4; ++k) {
        u32x4 aa = *(const u32x4*)(lds + L_CX2 + c16 * 272 + 64 * ((k + cq) & 3) + 16 * g);
#pragma unroll
        for (int j = 0; j < 4; ++j) oo[j] = mfma(aa, of[k][j], oo[j]);
      }
      float y[4];
#pragma unroll
      for (int j = 0; j < 4; ++j) y[j] = (oo[j][0] + oo[j][1]) + (oo[j][2] + oo[j][3]);
      u32x4 z;
#pragma unroll
      for (int j = 0; j < 4; ++j) z[j] = canon(__float_as_uint(sw16add(y[j], y[j])));
      if (!(g & 1)) cst128(rws, W_DP + (((g >> 1) * 128 + bid) * 512 + 64 * wave + 4 * c16) * 4, z);
    }
    TS(12);
    asm volatile(".Lmk_cf:");
    return;
  }
  {
    const int kq = wave & 3, hh = wave >> 2;
    asm volatile("s_waitcnt vmcnt(4)" ::: "memory");
    tag = mktag(ep);
    TS(2);
    bar();
    u32x4 kf[2][16], vf[4][4];
#pragma unroll
    for (int t = 0; t < 2; ++t)
#pragma unroll
      for (int s = 0; s < 16; ++s) { kf[t][s] = *(const u32x4*)(lds + (32 * kq + 16 * t + c16) * KVS + 64 * s + 16 * g); pin4(kf[t][s]); }
#pragma unroll
    for (int t = 0; t < 4; ++t)
#pragma unroll
      for (int k = 0; k < 4; ++k) {
        const char* p = lds + (32 * k + 4 * g + (c16 >> 2)) * KVS + (64 * wave + 16 * t + 4 * (c16 & 3)) * 2;
        u32x2 lo = trread(p), hi = trread(p + 16 * KVS); vf[t][k] = u32x4{lo[0], lo[1], hi[0], hi[1]}; pin4(vf[t][k]);
      }
    if (wave == 2) {
      _Pragma("nounroll") for (int it = 0; it < SPIN_MAX; ++it) { u32x2 v = cld64(rws, W_GATE + lane * 8); fence(); if (allw(v[0] == tag && v[1] == tag)) break; __builtin_amdgcn_s_sleep(2); }
    }
    waitvm0();
    asm volatile("" :: "v"(pfv));
    bar();
    if (tid == 128) cst32(rws, W_GATE2 + an * 4, tag);
    fence();
    TS(3);
    {
      const u32 qb = W_QB + (ab * 32 + 4 * wave) * 1024;
      poll_ne(rws, qb + (an & 15) * 256);
      TS(4);
      u32x4 v[4];
      _Pragma("nounroll") for (int it = 0; it < SPIN_MAX; ++it) {
#pragma unroll
        for (int i = 0; i < 4; ++i) v[i] = cld128(rws, qb + i * 1024 + lane * 16);
        fence();
        if (allw(ok4(v[0]) && ok4(v[1]) && ok4(v[2]) && ok4(v[3]))) break;
      }
#pragma unroll
      for (int i = 0; i < 4; ++i) *(u32x4*)(lds + (4 * wave + i) * 1040 + lane * 16) = v[i];
    }
    TS(5);
    bar();
    f32x4 a0 = Z4, a1 = Z4, b0 = Z4, b1 = Z4;
#pragma unroll
    for (int s = 0; s < 16; s += 2) {
      u32x4 q0 = *(const u32x4*)(lds + (16 * hh + c16) * 1040 + 64 * s + 16 * g), q1 = *(const u32x4*)(lds + (16 * hh + c16) * 1040 + 64 * s + 64 + 16 * g);
      a0 = mfma(kf[0][s], q0, a0); b0 = mfma(kf[1][s], q0, b0);
      a1 = mfma(kf[0][s + 1], q1, a1); b1 = mfma(kf[1][s + 1], q1, b1);
    }
    a0 += a1; b0 += b1;
    float m = fmaxf(fmaxf(fmaxf(a0[0], a0[1]), fmaxf(a0[2], a0[3])), fmaxf(fmaxf(b0[0], b0[1]), fmaxf(b0[2], b0[3])));
    m = sw16max(m); m = sw32max(m);
    if (lane < 16) ((float*)(lds + L_MX))[(16 * hh + lane) * 4 + kq] = m;
    bar();
    TS(6);
    {
      const f32x4 mx4 = *(const f32x4*)(lds + L_MX + (16 * hh + c16) * 16);
      const float ml = fmaxf(fmaxf(mx4[0], mx4[1]), fmaxf(mx4[2], mx4[3]));
      float p[8], l = 0.f;
#pragma unroll
      for (int i = 0; i < 8; ++i) { p[i] = __builtin_amdgcn_exp2f((i < 4 ? a0[i & 3] : b0[i & 3]) - ml); l += p[i]; }
      *(u32x4*)(lds + L_P + wave * 1024 + lane * 16) = u32x4{pk2(p[0], p[1]), pk2(p[2], p[3]), pk2(p[4], p[5]), pk2(p[6], p[7])};
      l = sw16add(l, l); l = sw32add(l, l);
      if (lane < 16) ((float*)(lds + L_LS))[(16 * hh + lane) * 4 + kq] = l;
    }
    bar();
    TS(7);
    if (wave == 0) {
      const int hd = lane & 31;
      const f32x4 l4 = *(const f32x4*)(lds + L_LS + hd * 16), mx4 = *(const f32x4*)(lds + L_MX + hd * 16);
      if (lane < 32) cst64(rws, W_LSI + (lane * 128 + ab * 64 + aks) * 8, u32x2{canon(__float_as_uint(fmaxf(fmaxf(mx4[0], mx4[1]), fmaxf(mx4[2], mx4[3])))), canon(__float_as_uint((l4[0] + l4[1]) + (l4[2] + l4[3])))});
      fence();
    }
#pragma unroll
    for (int t2 = 0; t2 < 2; ++t2) {
      f32x4 o[4] = {Z4, Z4, Z4, Z4};
#pragma unroll
      for (int k = 0; k < 4; ++k) {
        u32x4 pf = *(const u32x4*)(lds + L_P + (4 * t2 + k) * 1024 + lane * 16);
#pragma unroll
        for (int t = 0; t < 4; ++t) o[t] = mfma(pf, vf[t][k], o[t]);
      }
      const u32 off = W_CI + (16 * t2 + 4 * g + 32 * (wave >> 1)) * 32768 + (ab * 64 + aks) * 256 + (wave & 1) * 128 + c16 * 8;
#pragma unroll
      for (int r = 0; r < 4; ++r) __builtin_amdgcn_raw_buffer_store_b64(u32x2{canon(pk2(o[0][r], o[1][r])), canon(pk2(o[2][r], o[3][r]))}, rws, off + r * 32768, 0, CIAUX);
      fence();
    }
    fence();
    TS(8);
    __builtin_amdgcn_s_barrier();
    fence();
  }
  float* r2 = (float*)(lds + 40960);
  if (an < 64) {
    {
      const int col0 = 64 * ((an & 7) + 8 * (an >> 4)) + 32 * ((an >> 3) & 1), row = 64 * wave + 4 * (lane >> 2);
      u32x4 mr[4];
      __builtin_amdgcn_s_sleep(MRD);
      fence();
#pragma unroll
      for (int i = 0; i < 4; ++i) mr[i] = ld128(rm, ((row + i) * 3072 + 1024 + col0 + 8 * (lane & 3)) * 2);
      const u32x2 c0 = ld64(rc, row * 2), c1 = ld64(rc, 1024 + row * 2);
      fence();
      float v[2][8];
#pragma unroll
      for (int i = 0; i < 4; ++i) {
        const float s0 = silu(i & 1 ? bfhi(c0[i >> 1]) : bflo(c0[i >> 1])), s1 = silu(i & 1 ? bfhi(c1[i >> 1]) : bflo(c1[i >> 1]));
#pragma unroll
        for (int q = 0; q < 4; ++q) {
          const float lo = bflo(mr[i][q]), hi = bfhi(mr[i][q]);
          v[0][2 * q] = i ? fmaf(s0, lo, v[0][2 * q]) : s0 * lo; v[0][2 * q + 1] = i ? fmaf(s0, hi, v[0][2 * q + 1]) : s0 * hi;
          v[1][2 * q] = i ? fmaf(s1, lo, v[1][2 * q]) : s1 * lo; v[1][2 * q + 1] = i ? fmaf(s1, hi, v[1][2 * q + 1]) : s1 * hi;
        }
      }
      modred(v, red + wave * 64, lane);
      bar();
      if (wave == 0) {
        float s = 0.f;
#pragma unroll
        for (int w = 0; w < 8; ++w) s += red[w * 64 + lane];
        auto r = __builtin_amdgcn_permlane32_swap(__float_as_uint(s), __float_as_uint(s), false, false);
        if (lane < 32) cst64(rws, W_M2 + (col0 + lane) * 8, u32x2{tag, pk2(__uint_as_float(r[0]), __uint_as_float(r[1]))});
        fence();
      }
      TS(11);
    }
    const int rb = an >> 5, sl = an & 31;
    const u32 off = W_DP + ((rb * 128 + (tid >> 2)) * 512 + 16 * sl + 4 * (tid & 3)) * 4;
    TS(9);
    u32x4 v;
    _Pragma("nounroll") for (int it = 0; it < SPIN_MAX; ++it) { v = cld128(rws, off); fence(); if (allw(ok4(v))) break; }
    float z = sw16add(sw32add(__uint_as_float(v[0]), __uint_as_float(v[2])), sw32add(__uint_as_float(v[1]), __uint_as_float(v[3])));
    z += ror<8>(z); z += ror<4>(z);
    if (c16 < 4) r2[wave * 16 + 4 * c16 + g] = z;
    bar();
    if (tid < 16) {
      float s = 0.f;
#pragma unroll
      for (int w = 0; w < 8; ++w) s += r2[w * 16 + tid];
      cst64(rws, W_XE + ((16 * sl + tid) * 2 + rb) * 8, u32x2{tag, __float_as_uint(s)});
    }
    TS(10);
    asm volatile(".Lmk_cg:");
  } else if (an < 120) {
    const int rel = an - 64, fb = rel < 48 ? 2 * (rel & 7) + ((rel >> 3) & 1) + (rel & 48) : rel;
    const rsrc_t rd = mk(wd, 1792 * 512 * 2), rx = mk(x, 2048);
    const u16* wgu = c16 & 8 ? wu : wg;
    u32x2 r[2][8], dr[8];
    __builtin_amdgcn_s_sleep(FFD);
    fence();
#pragma unroll
    for (int t = 0; t < 2; ++t)
#pragma unroll
      for (int i = 0; i < 8; ++i) r[t][i] = *(const u32x2*)(wgu + (64 * wave + 32 * (t ^ (fb & 1)) + 8 * g + i) * 1792 + 32 * fb + 4 * (c16 & 7));
#pragma unroll
    for (int i = 0; i < 8; ++i) dr[i] = nt64(rd, ((32 * fb + 8 * g + i) * 512 + 64 * wave + 4 * c16) * 2);
    u32 xa = __builtin_amdgcn_raw_buffer_load_b16(rx, tid * 2, 0, 0), xb = __builtin_amdgcn_raw_buffer_load_b16(rx, 1024 + tid * 2, 0, 0);
    const u32x4 x0 = ld128(rx, lane * 16), x1 = ld128(rx, 1024 + lane * 16);
    u32x2 mg[4];
    _Pragma("nounroll") for (int it = 0; it < SPIN_MAX; ++it) {
#pragma unroll
      for (int i = 0; i < 4; ++i) mg[i] = cld64(rws, W_M2 + (i * 512 + tid) * 8);
      fence();
      if (allw(mg[0][0] == tag && mg[1][0] == tag && mg[2][0] == tag && mg[3][0] == tag)) break;
    }
    u32x4 gf[2][4], df[4];
    tr84(r[0], gf[0]); tr84(r[1], gf[1]); tr84(dr, df);
    float p0 = 0.f, p1 = 0.f;
#pragma unroll
    for (int q = 0; q < 4; ++q) { p0 += bflo(x0[q]) + bfhi(x0[q]); p1 += bflo(x1[q]) + bfhi(x1[q]); }
    p0 = wavesum(p0) * (1.f / 512.f); p1 = wavesum(p1) * (1.f / 512.f);
    float c0 = rbf(1.f + bflo(mg[2][1])), c1 = rbf(1.f + bfhi(mg[2][1])), ga0 = bflo(mg[0][1]), ga1 = bfhi(mg[0][1]);
    float sh0 = bflo(mg[1][1]), sh1 = bfhi(mg[1][1]), xf0 = bf2f(xa), xf1 = bf2f(xb);
    ((u32*)r2)[2048 + tid] = mg[3][1];
    pinf(p0); pinf(p1); pinf(c0); pinf(c1); pinf(ga0); pinf(ga1); pinf(sh0); pinf(sh1); pinf(xf0); pinf(xf1);
    TS(9);
    TS(10);
    u32x4 e;
    _Pragma("nounroll") for (int it = 0; it < SPIN_MAX; ++it) { e = cld128(rws, W_XE + tid * 16); fence(); if (allw(e[0] == tag && e[2] == tag)) break; }
    TS(13);
    const float x10 = rbf(xf0 + rbf(ga0 * rbf(__uint_as_float(e[1]))));
    const float x11 = rbf(xf1 + rbf(ga1 * rbf(__uint_as_float(e[3]))));
    {
      const float d0 = x10 - p0, d1 = x11 - p1;
      const float z = rowsum(sw16add(sw32add(d0, d1), sw32add(d0 * d0, d1 * d1)));
      if (c16 == 0) r2[g * 8 + wave] = z;
      r2[1024 + tid] = x10; r2[1536 + tid] = x11;
    }
    bar();
    {
      float st[4];
#pragma unroll
      for (int k = 0; k < 4; ++k) { const f32x4 u = *(const f32x4*)(r2 + 8 * k), w = *(const f32x4*)(r2 + 8 * k + 4); st[k] = (((u[0] + u[1]) + (u[2] + u[3])) + ((w[0] + w[1]) + (w[2] + w[3]))) * (1.f / 512.f); }
      const float ir0 = rcp(rbf(__builtin_amdgcn_sqrtf(rbf(rbf(fmaxf(st[1] - st[0] * st[0], 0.f)) + 1e-5f))));
      const float ir1 = rcp(rbf(__builtin_amdgcn_sqrtf(rbf(rbf(fmaxf(st[3] - st[2] * st[2], 0.f)) + 1e-5f))));
      const float h0 = rbf(rbf(rbf(x10 - rbf(p0 + st[0])) * ir0) * c0) + sh0, h1 = rbf(rbf(rbf(x11 - rbf(p1 + st[2])) * ir1) * c1) + sh1;
      const u32 hh = pk2(h0, h1);
      ((u16*)(lds + L_HS))[tid] = (u16)hh; ((u16*)(lds + L_HS))[512 + tid] = (u16)(hh >> 16);
    }
    bar();
    TS(14);
    {
      f32x4 acc[4] = {Z4, Z4, Z4, Z4};
#pragma unroll
      for (int t = 0; t < 2; ++t) {
        u32x4 a = *(const u32x4*)(lds + L_HS + ((lane & 1) * 512 + 64 * wave + 32 * (t ^ (fb & 1)) + 8 * g) * 2);
#pragma unroll
        for (int j = 0; j < 4; ++j) acc[j] = mfma(a, gf[t][j], acc[j]);
      }
      if (lane < 32) { f32x4 v = {acc[0][0], acc[1][0], acc[2][0], acc[3][0]}, w = {acc[0][1], acc[1][1], acc[2][1], acc[3][1]}; *(f32x4*)(red + wave * 128 + g * 64 + 4 * c16) = g ? w : v; }
    }
    bar();
    TS(15);
    {
      float gg = 0.f, uu = 0.f;
#pragma unroll
      for (int w = 0; w < 8; ++w) { gg += red[w * 128 + (lane >> 5) * 64 + (lane & 31)]; uu += red[w * 128 + (lane >> 5) * 64 + 32 + (lane & 31)]; }
      gg = rbf(gg); uu = rbf(uu);
      const float zz = 0.7978845608028654f * (gg + 0.044715f * gg * gg * gg);
      const float th = 1.f - 2.f * rcp(__expf(2.f * zz) + 1.f);
      const float act = rbf(0.5f * gg * (1.f + th)) * uu;
      u16* ap = (u16*)(r2 + 3072) + wave * 64;
      ap[lane] = (u16)pk2(act, 0.f);
      waitlgkm0();
      const u32x4 a = *(const u32x4*)(ap + (lane & 1) * 32 + 8 * g);
      f32x4 dd[4];
#pragma unroll
      for (int j = 0; j < 4; ++j) dd[j] = mfma(a, df[j], Z4);
      const int s = g & 1;
      u32x4 v = {canon(__float_as_uint(s ? dd[0][1] : dd[0][0])), canon(__float_as_uint(s ? dd[1][1] : dd[1][0])), canon(__float_as_uint(s ? dd[2][1] : dd[2][0])), canon(__float_as_uint(s ? dd[3][1] : dd[3][0]))};
      if (lane < 32) cst128(rws, W_FP + rel * 4096 + (g * 512 + 64 * wave + 4 * c16) * 4, v);
      fence();
    }
    TS(11);
    if (rel < 32) {
      const int rb = rel >> 4, sl = rel & 15;
      float z = 0.f;
      if (wave < 7) {
        const u32 off = W_FP + (tid >> 3) * 4096 + (rb * 512 + 32 * sl + 4 * (tid & 7)) * 4;
        u32x4 v;
        _Pragma("nounroll") for (int it = 0; it < SPIN_MAX; ++it) { v = cld128(rws, off); fence(); if (allw(ok4(v))) break; }
        z = sw16add(sw32add(__uint_as_float(v[0]), __uint_as_float(v[2])), sw32add(__uint_as_float(v[1]), __uint_as_float(v[3])));
        z += ror<8>(z);
      }
      if (!(lane & 8)) r2[256 + wave * 32 + 4 * (lane & 7) + g] = z;
      bar();
      if (tid < 32) {
        float s = 0.f;
#pragma unroll
        for (int w = 0; w < 8; ++w) s += r2[256 + w * 32 + tid];
        const int m = 32 * sl + tid;
        const u32 gbp = ((const u32*)r2)[2048 + m];
        const float gb = rb ? bfhi(gbp) : bflo(gbp);
        out[rb * 512 + m] = (u16)pk2(r2[1024 + rb * 512 + m] + rbf(gb * rbf(s)), 0.f);
        if (rel == 0 && tid == 0) cst32(rws, W_EPOCH, tag);
      }
      TS(12);
      asm volatile(".Lmk_ce:");
    }
  }
}

extern "C" void kernel_launch(void* const* d_in, const int* in_sizes, int n_in, void* d_out, int out_size, void* d_ws, size_t ws_size, hipStream_t stream) {
  mk_kernel<<<256, 512, 0, stream>>>((const u16*)d_in[0], (const u16*)d_in[1], (const u16*)d_in[2], (const u16*)d_in[3], (const u16*)d_in[4], (const u16*)d_in[5],
      (const u16*)d_in[7], (const u16*)d_in[8], (const u16*)d_in[9], (const u16*)d_in[10], (const u16*)d_in[11], (u16*)d_out, (char*)d_ws);
}
```
